```python
import torch

def _softmax(x, axis=-1):
    return torch.softmax(x, dim=axis)
def _gelu(x):
    return torch.nn.functional.gelu(x, approximate="tanh")
def _silu(x):
    return torch.nn.functional.silu(x)
def _sigmoid(x):
    return torch.sigmoid(x)
def _layernorm(x, axis=-1, eps=1e-5):
    m = x.mean(dim=axis, keepdim=True)
    v = x.var(dim=axis, keepdim=True, unbiased=False)
    return (x - m) / torch.sqrt(v + eps)
def _scatter_add(tgt, idx, src, axis=0):
    return tgt.clone().index_add_(axis, idx, src)
def _segment_sum(src, seg, n):
    out = torch.zeros((n,) + src.shape[1:], dtype=src.dtype)
    return out.index_add_(0, seg, src)
def _pad(x, pw):
    flat = tuple(v for pair in reversed(pw) for v in pair)
    return torch.nn.functional.pad(x, flat)

kernel_name = 'parallel_sum__spectral_fnet__geglu_ffn__prompt'

def setup_inputs():

    _w = (lambda a: (lambda t: t.to(torch.bfloat16) if t.is_floating_point() else t)(torch.as_tensor(a)))
    def _A_setup():
        B, S, D = 1, 2048, 512
        ns = torch.arange(S)[:, None] * torch.arange(S)[None, :]
        nd = torch.arange(D)[:, None] * torch.arange(D)[None, :]
        Ws_re = torch.cos(-2.0 * torch.pi * ns / S).to(float)
        Ws_im = torch.sin(-2.0 * torch.pi * ns / S).to(float)
        Wd_re = torch.cos(-2.0 * torch.pi * nd / D).to(float)
        Wd_im = torch.sin(-2.0 * torch.pi * nd / D).to(float)
        return {
            "x": _w((lambda *s: torch.randn(*s, dtype=torch.bfloat16) * 0.05)(B, S, D)),
            "dft_seq_re": _w(Ws_re),
            "dft_seq_im": _w(Ws_im),
            "dft_hid_re": _w(Wd_re),
            "dft_hid_im": _w(Wd_im),
        }
    _A_in = _A_setup()

    def _B_setup():
        B, S, D, F = 1, 2048, 512, 1792
        return {
            "x": _w((lambda *s: torch.randn(*s, dtype=torch.bfloat16) * 0.05)(B, S, D)),
            "w_gate": _w((lambda *s: torch.randn(*s, dtype=torch.bfloat16) * 0.2)(D, F)),
            "w_up": _w((lambda *s: torch.randn(*s, dtype=torch.bfloat16) * 0.2)(D, F)),
            "w_down": _w((lambda *s: torch.randn(*s, dtype=torch.bfloat16) * 0.2)(F, D)),
        }
    _B_in = _B_setup()

    return {
        "A_x": _A_in["x"],
        "A_dft_seq_re": _A_in["dft_seq_re"],
        "A_dft_seq_im": _A_in["dft_seq_im"],
        "A_dft_hid_re": _A_in["dft_hid_re"],
        "A_dft_hid_im": _A_in["dft_hid_im"],
        "B_x": _B_in["x"],
        "B_w_gate": _B_in["w_gate"],
        "B_w_up": _B_in["w_up"],
        "B_w_down": _B_in["w_down"],
    }

def reference(A_x, A_dft_seq_re, A_dft_seq_im, A_dft_hid_re, A_dft_hid_im, B_x, B_w_gate, B_w_up, B_w_down):
    def _A_ref(x, dft_seq_re, dft_seq_im, dft_hid_re, dft_hid_im):
        a = torch.einsum("bsd,ed->bse", x, dft_hid_re)
        b = torch.einsum("bsd,ed->bse", x, dft_hid_im)
        y = torch.einsum("ts,bse->bte", dft_seq_re, a) - torch.einsum("ts,bse->bte", dft_seq_im, b)
        return y

    def _B_ref(x, w_gate, w_up, w_down):
        return (_gelu(x @ w_gate) * (x @ w_up)) @ w_down

    x = A_x
    xn = _layernorm(x, axis=-1)
    return x + _A_ref(xn, A_dft_seq_re, A_dft_seq_im, A_dft_hid_re, A_dft_hid_im) + _B_ref(xn, B_w_gate, B_w_up, B_w_down)
```

```cpp
#include <hip/hip_runtime.h>
#include <hip/hip_bf16.h>
typedef unsigned short u16;
typedef short bf16x8 __attribute__((ext_vector_type(8)));
typedef float f32x4 __attribute__((ext_vector_type(4)));
constexpr int S=2048, D=512, F=1792;
__device__ __forceinline__ float bf2f(u16 v){ return __uint_as_float(((unsigned)v)<<16); }
__device__ __forceinline__ u16 f2bf(float f){ __hip_bfloat16 b=__float2bfloat16(f); return *(u16*)&b; }
__device__ __forceinline__ float rbf(float f){ return bf2f(f2bf(f)); }

__global__ void ln_k(const u16* x, u16* xn){
  int row=blockIdx.x, l=threadIdx.x;
  float v[8]; float s=0;
  for(int j=0;j<8;++j){ v[j]=bf2f(x[row*D+l*8+j]); s+=v[j]; }
  for(int o=32;o;o>>=1) s+=__shfl_xor(s,o);
  float m=s*(1.f/D); float q=0;
  for(int j=0;j<8;++j){ float d=v[j]-m; q+=d*d; }
  for(int o=32;o;o>>=1) q+=__shfl_xor(q,o);
  float mb=rbf(m), vb=rbf(q*(1.f/D));
  float sd=rbf(sqrtf(rbf(vb+1e-5f)));
  for(int j=0;j<8;++j) xn[row*D+l*8+j]=f2bf(rbf(v[j]-mb)/sd);
}
__global__ void transpose_k(const u16* in, u16* out, int R, int C){
  int i=blockIdx.x*256+threadIdx.x; if(i>=R*C) return; int r=i/C,c=i%C; out[c*R+r]=in[i];
}
__device__ __forceinline__ f32x4 dot16(const u16* A,int lda,const u16* B,int ldb,int K,int lane,f32x4 acc){
  int r=lane&15,q=lane>>4;
  for(int k=0;k<K;k+=32){
    bf16x8 a=*(const bf16x8*)(A+r*lda+k+q*8), b=*(const bf16x8*)(B+r*ldb+k+q*8);
    acc=__builtin_amdgcn_mfma_f32_16x16x32_bf16(a,b,acc,0,0,0);
  }
  return acc;
}
__global__ void s1_ab(const u16* xn,const u16* wr,const u16* wi,u16* abT){
  int lane=threadIdx.x; int mt=blockIdx.x, nt=blockIdx.y;
  const u16* W = nt<32 ? wr+nt*16*D : wi+(nt-32)*16*D;
  f32x4 acc={0,0,0,0}; acc=dot16(xn+mt*16*D,D,W,D,D,lane,acc);
  float sg = nt<32?1.f:-1.f;
  for(int j=0;j<4;++j){ int m=mt*16+(lane>>4)*4+j, n=nt*16+(lane&15); abT[n*S+m]=f2bf(sg*acc[j]); }
}
__global__ void s1_h(const u16* xn,const u16* wgT,const u16* wuT,u16* h){
  int lane=threadIdx.x; int mt=blockIdx.x, nt=blockIdx.y;
  f32x4 g={0,0,0,0},u={0,0,0,0};
  g=dot16(xn+mt*16*D,D,wgT+nt*16*D,D,D,lane,g); u=dot16(xn+mt*16*D,D,wuT+nt*16*D,D,D,lane,u);
  for(int j=0;j<4;++j){ int m=mt*16+(lane>>4)*4+j, n=nt*16+(lane&15);
    float gg=rbf(g[j]), uu=rbf(u[j]);
    float ge=rbf(0.5f*gg*(1.f+tanhf(0.7978845608f*(gg+0.044715f*gg*gg*gg))));
    h[m*F+n]=f2bf(ge*uu); }
}
__global__ void s2(const u16* x,const u16* cr,const u16* ci,const u16* abT,const u16* h,const u16* wdT,u16* out){
  int lane=threadIdx.x; int mt=blockIdx.x, nt=blockIdx.y;
  f32x4 acc={0,0,0,0};
  acc=dot16(cr+mt*16*S,S,abT+nt*16*S,S,S,lane,acc);
  acc=dot16(ci+mt*16*S,S,abT+(512+nt*16)*S,S,S,lane,acc);
  acc=dot16(h+mt*16*F,F,wdT+nt*16*F,F,F,lane,acc);
  for(int j=0;j<4;++j){ int m=mt*16+(lane>>4)*4+j, n=nt*16+(lane&15); out[m*D+n]=f2bf(acc[j]+bf2f(x[m*D+n])); }
}
extern "C" void kernel_launch(void* const* d_in, const int* in_sizes, int n_in, void* d_out, int out_size, void* d_ws, size_t ws_size, hipStream_t stream){
  const u16* x=(const u16*)d_in[0]; const u16* cr=(const u16*)d_in[1]; const u16* ci=(const u16*)d_in[2];
  const u16* wr=(const u16*)d_in[3]; const u16* wi=(const u16*)d_in[4];
  const u16* wg=(const u16*)d_in[6]; const u16* wu=(const u16*)d_in[7]; const u16* wd=(const u16*)d_in[8];
  u16* ws=(u16*)d_ws; u16* xn=ws; u16* abT=xn+S*D; u16* h=abT+1024*S; u16* wgT=h+S*F; u16* wuT=wgT+D*F; u16* wdT=wuT+D*F;
  ln_k<<<S,64,0,stream>>>(x,xn);
  transpose_k<<<D*F/256,256,0,stream>>>(wg,wgT,D,F);
  transpose_k<<<D*F/256,256,0,stream>>>(wu,wuT,D,F);
  transpose_k<<<D*F/256,256,0,stream>>>(wd,wdT,F,D);
  s1_ab<<<dim3(S/16,64),64,0,stream>>>(xn,wr,wi,abT);
  s1_h<<<dim3(S/16,F/16),64,0,stream>>>(xn,wgT,wuT,h);
  s2<<<dim3(S/16,D/16),64,0,stream>>>(x,cr,ci,abT,h,wdT,(u16*)d_out);
}
```

```cpp
#include <hip/hip_runtime.h>
#include <hip/hip_bf16.h>
typedef unsigned short u16;
typedef short bf16x8 __attribute__((ext_vector_type(8)));
typedef float f32x4 __attribute__((ext_vector_type(4)));
constexpr int S=2048, D=512, F=1792;
__device__ __forceinline__ float bf2f(u16 v){ return __uint_as_float(((unsigned)v)<<16); }
__device__ __forceinline__ u16 f2bf(float f){ __hip_bfloat16 b=__float2bfloat16(f); return *(u16*)&b; }
__device__ __forceinline__ float rbf(float f){ return bf2f(f2bf(f)); }

__global__ void ln_k(const u16* x, u16* xn){
  int row=blockIdx.x, l=threadIdx.x;
  float v[8]; float s=0;
  for(int j=0;j<8;++j){ v[j]=bf2f(x[row*D+l*8+j]); s+=v[j]; }
  for(int o=32;o;o>>=1) s+=__shfl_xor(s,o);
  float m=s*(1.f/D); float q=0;
  for(int j=0;j<8;++j){ float d=v[j]-m; q+=d*d; }
  for(int o=32;o;o>>=1) q+=__shfl_xor(q,o);
  float mb=rbf(m), vb=rbf(q*(1.f/D));
  float sd=rbf(sqrtf(rbf(vb+1e-5f)));
  for(int j=0;j<8;++j) xn[row*D+l*8+j]=f2bf(rbf(v[j]-mb)/sd);
}
__global__ void transpose_k(const u16* in, u16* out, int R, int C){
  int i=blockIdx.x*256+threadIdx.x; if(i>=R*C) return; int r=i/C,c=i%C; out[c*R+r]=in[i];
}
__device__ __forceinline__ f32x4 dot16(const u16* A,int lda,const u16* B,int ldb,int K,int lane,f32x4 acc){
  int r=lane&15,q=lane>>4;
  for(int k=0;k<K;k+=32){
    bf16x8 a=*(const bf16x8*)(A+r*lda+k+q*8), b=*(const bf16x8*)(B+r*ldb+k+q*8);
    acc=__builtin_amdgcn_mfma_f32_16x16x32_bf16(a,b,acc,0,0,0);
  }
  return acc;
}
__global__ void s1_ab(const u16* xn,const u16* wr,const u16* wi,u16* abT){
  int lane=threadIdx.x; int mt=blockIdx.x, nt=blockIdx.y;
  const u16* W = nt<32 ? wr+nt*16*D : wi+(nt-32)*16*D;
  f32x4 acc={0,0,0,0}; acc=dot16(xn+mt*16*D,D,W,D,D,lane,acc);
  float sg = nt<32?1.f:-1.f;
  for(int j=0;j<4;++j){ int m=mt*16+(lane>>4)*4+j, n=nt*16+(lane&15); abT[n*S+m]=f2bf(sg*acc[j]); }
}
__global__ void s1_h(const u16* xn,const u16* wgT,const u16* wuT,u16* h){
  int lane=threadIdx.x; int mt=blockIdx.x, nt=blockIdx.y;
  f32x4 g={0,0,0,0},u={0,0,0,0};
  g=dot16(xn+mt*16*D,D,wgT+nt*16*D,D,D,lane,g); u=dot16(xn+mt*16*D,D,wuT+nt*16*D,D,D,lane,u);
  for(int j=0;j<4;++j){ int m=mt*16+(lane>>4)*4+j, n=nt*16+(lane&15);
    float gg=rbf(g[j]), uu=rbf(u[j]);
    float ge=rbf(0.5f*gg*(1.f+tanhf(0.7978845608f*(gg+0.044715f*gg*gg*gg))));
    h[m*F+n]=f2bf(ge*uu); }
}
__global__ void s2(const u16* x,const u16* cr,const u16* ci,const u16* abT,const u16* h,const u16* wdT,u16* out){
  int lane=threadIdx.x; int mt=blockIdx.x, nt=blockIdx.y;
  f32x4 acc={0,0,0,0};
  acc=dot16(cr+mt*16*S,S,abT+nt*16*S,S,S,lane,acc);
  acc=dot16(ci+mt*16*S,S,abT+(512+nt*16)*S,S,S,lane,acc);
  acc=dot16(h+mt*16*F,F,wdT+nt*16*F,F,F,lane,acc);
  for(int j=0;j<4;++j){ int m=mt*16+(lane>>4)*4+j, n=nt*16+(lane&15); out[m*D+n]=f2bf(acc[j]+bf2f(x[m*D+n])); }
}

typedef unsigned u32x4 __attribute__((ext_vector_type(4)));
__global__ __launch_bounds__(256) void probe_k(const char* p,int bytes,unsigned long long* ts){
  unsigned long long t0=__builtin_amdgcn_s_memrealtime();
  const char* a=p+(size_t)blockIdx.x*bytes+threadIdx.x*16;
  u32x4 acc={0,0,0,0}; u32x4 v[16];
  #pragma unroll
  for(int i=0;i<16;++i){ v[i]=*(const u32x4*)a; a+=4096; }
  for(int it=16;it<bytes/4096;it+=16){
    #pragma unroll
    for(int i=0;i<16;++i){ acc^=v[i]; v[i]=*(const u32x4*)a; a+=4096; }
  }
  #pragma unroll
  for(int i=0;i<16;++i) acc^=v[i];
  unsigned long long extra=0;
  if((acc.x^acc.y^acc.z^acc.w)==0x12345678u) extra=1;
  __syncthreads();
  if(threadIdx.x==0){ unsigned long long t1=__builtin_amdgcn_s_memrealtime(); ts[blockIdx.x]=t1+extra-t0; }
}
__global__ void spin_k(const unsigned long long* ts,int n){
  unsigned long long sum=0;
  for(int i=0;i<n;++i) sum+=ts[i];
  unsigned long long span=sum/n; if(span>5000) span=5000;
  unsigned long long t0=__builtin_amdgcn_s_memrealtime();
  while(__builtin_amdgcn_s_memrealtime()-t0 < span*20) __builtin_amdgcn_s_sleep(8);
}
extern "C" void kernel_launch(void* const* d_in, const int* in_sizes, int n_in, void* d_out, int out_size, void* d_ws, size_t ws_size, hipStream_t stream){
  const u16* x=(const u16*)d_in[0]; const u16* cr=(const u16*)d_in[1]; const u16* ci=(const u16*)d_in[2];
  const u16* wr=(const u16*)d_in[3]; const u16* wi=(const u16*)d_in[4];
  const u16* wg=(const u16*)d_in[6]; const u16* wu=(const u16*)d_in[7]; const u16* wd=(const u16*)d_in[8];
  u16* ws=(u16*)d_ws; u16* xn=ws; u16* abT=xn+S*D; u16* h=abT+1024*S; u16* wgT=h+S*F; u16* wuT=wgT+D*F; u16* wdT=wuT+D*F;
  unsigned long long* ts=(unsigned long long*)((char*)d_ws+128*1024*1024);
  probe_k<<<7,256,0,stream>>>((const char*)wd,262144,ts);
  spin_k<<<1,1,0,stream>>>(ts,7);
  ln_k<<<S,64,0,stream>>>(x,xn);
  transpose_k<<<D*F/256,256,0,stream>>>(wg,wgT,D,F);
  transpose_k<<<D*F/256,256,0,stream>>>(wu,wuT,D,F);
  transpose_k<<<D*F/256,256,0,stream>>>(wd,wdT,F,D);
  s1_ab<<<dim3(S/16,64),64,0,stream>>>(xn,wr,wi,abT);
  s1_h<<<dim3(S/16,F/16),64,0,stream>>>(xn,wgT,wuT,h);
  s2<<<dim3(S/16,D/16),64,0,stream>>>(x,cr,ci,abT,h,wdT,(u16*)d_out);
}
```

```cpp
#include <hip/hip_runtime.h>
#include <hip/hip_bf16.h>

typedef unsigned short u16;
typedef short bf16x8 __attribute__((ext_vector_type(8)));
typedef short s16x4 __attribute__((ext_vector_type(4)));
typedef float f32x4 __attribute__((ext_vector_type(4)));
typedef float f32x2 __attribute__((ext_vector_type(2)));
typedef unsigned u32x4 __attribute__((ext_vector_type(4)));
typedef unsigned u32x2 __attribute__((ext_vector_type(2)));
typedef __bf16 bfx2 __attribute__((ext_vector_type(2)));
#define LAS __attribute__((address_space(3)))
#define GAS __attribute__((address_space(1)))
#define DI __device__ __forceinline__

constexpr int S = 2048, D = 512, F = 1792;
constexpr size_t WS_HF = 0;
constexpr size_t WS_AB = WS_HF + (size_t)S * F * 2;
constexpr size_t WS_P = WS_AB + (size_t)2 * D * S * 2;
constexpr size_t WS_FLAG = WS_P + (size_t)64 * 16 * 16384;
constexpr size_t WS_TS = WS_FLAG + 65536;

#define TS(k)
#define WAIT_V(n) asm volatile("s_waitcnt vmcnt(%0)" ::"n"(n) : "memory")
#define BAR_L() asm volatile("s_waitcnt lgkmcnt(0)\n\ts_barrier" ::: "memory")
#define BAR() asm volatile("s_barrier" ::: "memory")
#define SCHED() __builtin_amdgcn_sched_barrier(0)
#define MFMA(a, b, c) __builtin_amdgcn_mfma_f32_16x16x32_bf16((a), (b), (c), 0, 0, 0)

DI void glds16(const char* sbase, unsigned voff, unsigned lds) {
  unsigned keep;
  asm volatile("s_mov_b32 %0, m0\n\ts_mov_b32 m0, %3\n\ts_nop 0\n\tglobal_load_lds_dwordx4 %1, %2\n\ts_mov_b32 m0, %0"
               : "=&s"(keep) : "v"(voff), "s"(sbase), "s"(lds) : "memory");
}
DI void glds4(const char* sbase, unsigned voff, unsigned lds) {
  unsigned keep;
  asm volatile("s_mov_b32 %0, m0\n\ts_mov_b32 m0, %3\n\ts_nop 0\n\tglobal_load_lds_dword %1, %2\n\ts_mov_b32 m0, %0"
               : "=&s"(keep) : "v"(voff), "s"(sbase), "s"(lds) : "memory");
}
DI bf16x8 lds128(unsigned a) { return *(const LAS bf16x8*)a; }
DI bf16x8 ldstr(unsigned a, int off2) {
  s16x4 lo = __builtin_amdgcn_ds_read_tr16_b64_v4i16((LAS s16x4*)a);
  s16x4 hi = __builtin_amdgcn_ds_read_tr16_b64_v4i16((LAS s16x4*)(a + off2));
  return __builtin_shufflevector(lo, hi, 0, 1, 2, 3, 4, 5, 6, 7);
}
DI float dot2(unsigned a, unsigned b, float c) { return __builtin_amdgcn_fdot2_f32_bf16(__builtin_bit_cast(bfx2, a), __builtin_bit_cast(bfx2, b), c, false); }
DI unsigned pkbf(float lo, float hi) { f32x2 f = {lo, hi}; return __builtin_bit_cast(unsigned, __builtin_convertvector(f, bfx2)); }
DI float rbf(float f) { return __uint_as_float(pkbf(f, 0.f) << 16); }
DI float gelu_mul(float g, float u) {
  float e = __builtin_amdgcn_exp2f(g * (-2.3022082f + -0.10294324f * g * g));
  return g * __builtin_amdgcn_rcpf(1.0f + e) * u;
}

constexpr int K1_SLOT = 53248, K1_NST = 3, K1_TAB = K1_SLOT * K1_NST, K1_LDS = 163840, K1_NT = 8;

template <int NG>
DI void k1_body(const char* x, const char* wr, const char* wi, const char* wg, const char* wu, char* ws, int rb, int g, int j, int wid, int lane) {
  constexpr int NA = 9 - 2 * NG;
  constexpr int GUB = NG * 8192;
  constexpr int ABO = 16384 + GUB;
  const int f0 = 448 * g + (NG == 4 ? 128 * j : 384);
  const int e0 = 256 * (g & 1) + (NG == 4 ? 160 + 32 * j : 0);
  LAS float* tab = (LAS float*)K1_TAB;
  if (wid >= 4) {
    const int w = wid - 4, lr = lane >> 3, lc = lane & 7;
    const unsigned offA = lr * 1024 + ((lc ^ ((w << 1) | ((lr >> 1) & 1))) << 4);
    const unsigned offG = lr * (F * 2) + ((lc ^ ((((lr >> 1) & 1) | ((w & 1) << 1)) << 1)) << 4);
    const unsigned offE = lr * 1024 + ((lc ^ (((w & 1) << 2) | (lr >> 1))) << 4);
    const char* xa = x + (size_t)(rb * 128 + 8 * w) * 1024;
    const char* ga = wg + (size_t)(8 * w) * (F * 2) + f0 * 2;
    const char* ua = wu + (size_t)(8 * w) * (F * 2) + f0 * 2;
    const char* ea = (g < 2 ? wr : wi) + (size_t)(e0 + 8 * w) * 1024;
    auto stage = [&](int kt, unsigned slot) {
      const unsigned d = slot + w * 1024;
#pragma unroll
      for (int i = 0; i < 4; ++i) glds16(xa + i * 32768 + kt * 128, offA, d + i * 4096);
#pragma unroll
      for (int i = 0; i < 2 * NG; ++i) {
        const int sub = i >> 1;
        const char* b = (NG == 4 ? (sub < 2 ? ga : ua) + (sub & 1) * 128 : (sub ? ua : ga));
        glds16(b + (size_t)(kt * 64 + (i & 1) * 32) * (F * 2), offG, d + 16384 + i * 4096);
      }
#pragma unroll
      for (int i = 0; i < NA; ++i) glds16(ea + i * 32768 + kt * 128, offE, d + ABO + i * 4096);
    };
    float sm[4] = {}, sq[4] = {}, cs[NG] = {}, ca[NA] = {};
    const int gq = lane >> 4, qq = (lane & 15) >> 2, p = lane & 3, sw2 = ((qq >> 1) & 1) | ((gq & 1) << 1);
    stage(0, 0); stage(1, K1_SLOT);
    unsigned cur = 0, nxt = 2 * K1_SLOT;
    for (int t = 0; t < K1_NT; ++t) {
      if (t + 1 < K1_NT) WAIT_V(13); else WAIT_V(0);
      BAR_L();
      if (t + 2 < K1_NT) stage(t + 2, nxt);
#pragma unroll
      for (int c = 0; c < 4; ++c) {
        u32x4 v = __builtin_bit_cast(u32x4, lds128(cur + w * 4096 + c * 1024 + lane * 16));
#pragma unroll
        for (int e = 0; e < 4; ++e) { sm[c] = dot2(v[e], 0x3f803f80u, sm[c]); sq[c] = dot2(v[e], v[e], sq[c]); }
      }
#pragma unroll
      for (int c = 0; c < NG; ++c) {
        const int ct = NG == 4 ? c : 2 * (w & 1) + c, sub = NG == 4 ? w : w >> 1;
        const unsigned a = cur + 16384 + sub * 8192 + (8 * gq + qq) * 128 + ((ct ^ sw2) << 5) + 8 * p;
#pragma unroll
        for (int kk = 0; kk < 2; ++kk) {
          u32x4 v = __builtin_bit_cast(u32x4, ldstr(a + kk * 4096, 512));
#pragma unroll
          for (int e = 0; e < 4; ++e) cs[c] = dot2(v[e], 0x3f803f80u, cs[c]);
        }
      }
#pragma unroll
      for (int i = 0; i < NA; ++i) {
        u32x4 v = __builtin_bit_cast(u32x4, lds128(cur + ABO + w * 1024 + i * 4096 + lane * 16));
#pragma unroll
        for (int e = 0; e < 4; ++e) ca[i] = dot2(v[e], 0x3f803f80u, ca[i]);
      }
      nxt = cur; cur = cur + K1_SLOT == K1_TAB ? 0 : cur + K1_SLOT;
    }
#pragma unroll
    for (int c = 0; c < 4; ++c) {
      float s = sm[c], q = sq[c];
      s += __shfl_xor(s, 1); q += __shfl_xor(q, 1); s += __shfl_xor(s, 2); q += __shfl_xor(q, 2); s += __shfl_xor(s, 4); q += __shfl_xor(q, 4);
      const float mean = s * (1.0f / D), var = fmaxf(q * (1.0f / D) - mean * mean, 0.f);
      const float sd = rbf(sqrtf(rbf(rbf(var) + 1e-5f)));
      if (lc == 0) { tab[32 * w + 8 * c + lr] = rbf(mean); tab[128 + 32 * w + 8 * c + lr] = 1.0f / sd; }
    }
#pragma unroll
    for (int c = 0; c < NG; ++c) {
      const int ct = NG == 4 ? c : 2 * (w & 1) + c, sub = NG == 4 ? w : w >> 1;
      float s = cs[c]; s += __shfl_xor(s, 16); s += __shfl_xor(s, 32);
      if (lane < 16) tab[256 + sub * 64 + ct * 16 + lane] = s;
    }
#pragma unroll
    for (int i = 0; i < NA; ++i) {
      float s = ca[i]; s += __shfl_xor(s, 1); s += __shfl_xor(s, 2); s += __shfl_xor(s, 4);
      if (lc == 0) tab[256 + NG * 64 + 8 * (w + 4 * i) + lr] = s;
    }
    BAR_L();
  } else {
    const int wm = wid >> 1, wn = wid & 1, i16 = lane & 15, q = lane >> 4, qq = i16 >> 2, p = lane & 3;
    const int sw2 = ((qq >> 1) & 1) | ((q & 1) << 1);
    const unsigned bx = (64 * wm + 8 * qq + p) * 128 + ((q ^ (i16 >> 1)) << 4);
    const unsigned be = ABO + (16 * NA * wn + i16) * 128 + ((q ^ (i16 >> 1)) << 4);
    unsigned bg[NG];
#pragma unroll
    for (int c = 0; c < NG; ++c) bg[c] = 16384 + (NG == 4 ? wn * 8192 : 0) + (8 * q + qq) * 128 + (((NG == 4 ? c : 2 * wn + c) ^ sw2) << 5) + 8 * p;
    constexpr int UPO = GUB / 2;
    f32x4 ag[4][NG] = {}, au[4][NG] = {}, aa[4][NA] = {};
    unsigned cur = 0;
    for (int t = 0; t < K1_NT; ++t) {
      BAR_L();
      if (wid == 0 && t == 0) TS(1);
#pragma unroll
      for (int kk = 0; kk < 2; ++kk) {
        bf16x8 xf[4];
#pragma unroll
        for (int m = 0; m < 4; ++m) xf[m] = lds128(cur + (bx ^ (kk << 6)) + (m >> 1) * 4096 + (m & 1) * 512);
#pragma unroll
        for (int c = 0; c < NG; ++c) {
          bf16x8 wf = ldstr(cur + bg[c] + kk * 4096, 512);
#pragma unroll
          for (int m = 0; m < 4; ++m) ag[m][c] = MFMA(wf, xf[m], ag[m][c]);
          bf16x8 uf = ldstr(cur + bg[c] + UPO + kk * 4096, 512);
#pragma unroll
          for (int m = 0; m < 4; ++m) au[m][c] = MFMA(uf, xf[m], au[m][c]);
        }
#pragma unroll
        for (int a = 0; a < NA; ++a) {
          bf16x8 ef = lds128(cur + (be ^ (kk << 6)) + a * 2048);
#pragma unroll
          for (int m = 0; m < 4; ++m) aa[m][a] = MFMA(xf[m], ef, aa[m][a]);
        }
      }
      cur = cur + K1_SLOT == K1_TAB ? 0 : cur + K1_SLOT;
    }
    if (wid == 0) TS(2);
    BAR_L();
    if (wid == 0) TS(3);
    const float sgn = g < 2 ? 1.0f : -1.0f;
#pragma unroll
    for (int sb = 0; sb < 2; ++sb) {
      const int s0 = 64 * wm + 32 * sb + 8 * q;
      const f32x4 mb0 = *(LAS f32x4*)(tab + s0), mb1 = *(LAS f32x4*)(tab + s0 + 4);
      const f32x4 r0 = *(LAS f32x4*)(tab + 128 + s0) * sgn, r1 = *(LAS f32x4*)(tab + 128 + s0 + 4) * sgn;
#pragma unroll
      for (int a = 0; a < NA; ++a) {
        const int tile = NA * wn + a;
        const float c = tab[256 + NG * 64 + 16 * tile + i16];
        const f32x4 v0 = (aa[2 * sb][a] - mb0 * c) * r0, v1 = (aa[2 * sb + 1][a] - mb1 * c) * r1;
        u32x4 o = {pkbf(v0[0], v0[1]), pkbf(v0[2], v0[3]), pkbf(v1[0], v1[1]), pkbf(v1[2], v1[3])};
        const int eb = (e0 >> 4) + tile;
        char* dst = ws + WS_AB + (size_t)(((((g >> 1) * 4 + (eb >> 3)) * 32 + 2 * rb + wm) * 8 + (eb & 7)) * 2 + sb) * 1024 + lane * 16;
        *(u32x4*)dst = o;
      }
    }
#pragma unroll
    for (int m = 0; m < 4; ++m) {
      const int sl = 64 * wm + 32 * (m >> 1) + 4 * (m & 1) + 8 * qq + p;
      const float mb = tab[sl], r = tab[128 + sl];
#pragma unroll
      for (int fb = 0; fb < NG / 2; ++fb) {
        float hv[8];
#pragma unroll
        for (int c = 0; c < 2; ++c) {
          const int fo = 16 * NG * wn + 32 * fb + 16 * c + 4 * q;
          const f32x4 cg = *(LAS f32x4*)(tab + 256 + fo), cu = *(LAS f32x4*)(tab + 256 + NG * 32 + fo);
          const f32x4 gv = (ag[m][2 * fb + c] - cg * mb) * r, uv = (au[m][2 * fb + c] - cu * mb) * r;
#pragma unroll
          for (int e = 0; e < 4; ++e) hv[4 * c + e] = gelu_mul(gv[e], uv[e]);
        }
        u32x4 o = {pkbf(hv[0], hv[1]), pkbf(hv[2], hv[3]), pkbf(hv[4], hv[5]), pkbf(hv[6], hv[7])};
        const int kstep = 7 * g + (NG == 4 ? 2 * j + wn : 6), fblk = NG == 4 ? fb : wn;
        char* dst = ws + WS_HF + (size_t)((((rb * 28 + kstep) * 4 + 2 * wm + (m >> 1)) * 2 + (m & 1)) * 2 + fblk) * 1024 + lane * 16;
        *(u32x4*)dst = o;
      }
    }
  }
}

__global__ __launch_bounds__(512, 2) void k1(const char* x, const char* wr, const char* wi, const char* wg, const char* wu, char* ws) {
  extern __shared__ __attribute__((aligned(16))) char shm[];
  const int b = blockIdx.x, xc = b & 7, r = b >> 3, g = xc >> 1, rb = (xc & 1) * 8 + (r >> 2), j = r & 3;
  const int wid = __builtin_amdgcn_readfirstlane(threadIdx.x >> 6), lane = threadIdx.x & 63;
  if (wid == 0) TS(0);
  if (threadIdx.x == 0) *(unsigned*)(ws + WS_FLAG + b * 256) = 0u;
  if (j < 3) k1_body<4>(x, wr, wi, wg, wu, ws, rb, g, j, wid, lane);
  else k1_body<2>(x, wr, wi, wg, wu, ws, rb, g, j, wid, lane);
  if (wid == 0) { TS(4); WAIT_V(0); TS(5); }
}

constexpr int K2_SLOT = 32768, K2_NST = 4, K2_LDS = K2_SLOT * K2_NST, K2_NF = 7, K2_NS = 16, K2_NT = K2_NF + K2_NS;

__global__ __launch_bounds__(512, 2) void k2(const char* x, const char* cr, const char* ci, const char* wd, char* ws, char* out) {
  extern __shared__ __attribute__((aligned(16))) char shm[];
  const int b = blockIdx.x, xc = b & 7, r = b >> 3, ks = xc >> 1, tm = (xc & 1) * 8 + (r >> 2), tn = r & 3;
  const int wid = __builtin_amdgcn_readfirstlane(threadIdx.x >> 6), lane = threadIdx.x & 63;
  const int tile = tm * 4 + tn;
  if (wid == 0) TS(6);
  GAS unsigned* flags = (GAS unsigned*)(ws + WS_FLAG);
  if (wid >= 4) {
    const int w = wid - 4, lr = lane >> 3, lc = lane & 7;
    const unsigned offC = lr * 4096 + ((lc ^ ((w << 1) | ((lr >> 1) & 1))) << 4);
    const unsigned offW = lr * 1024 + ((lc ^ (((lr >> 1) & 3) << 1)) << 4);
    const unsigned offL = lane * 16;
    const char* ca = (ks < 2 ? cr : ci) + (size_t)(tm * 128 + 8 * w) * 4096 + (ks & 1) * 2048;
    const char* ab = ws + WS_AB + (size_t)(((ks >> 1) * 4 + tn) * 32 + (ks & 1) * 16) * 16384 + w * 1024;
    const char* ha = ws + WS_HF + (size_t)(tm * 28 + ks * 7) * 16384 + w * 1024;
    const char* wa = wd + (size_t)(ks * 448 + 8 * w) * 1024 + tn * 256;
    auto stage = [&](int kt, unsigned slot) {
      const unsigned d = slot + w * 1024;
      if (kt < K2_NF) {
#pragma unroll
        for (int i = 0; i < 4; ++i) glds16(ha + kt * 16384 + i * 4096, offL, d + i * 4096);
#pragma unroll
        for (int i = 0; i < 4; ++i) glds16(wa + (kt * 64 + (i & 1) * 32) * 1024 + (i >> 1) * 128, offW, d + 16384 + i * 4096);
      } else {
        const int k = kt - K2_NF;
#pragma unroll
        for (int i = 0; i < 4; ++i) glds16(ca + i * 131072 + k * 128, offC, d + i * 4096);
#pragma unroll
        for (int i = 0; i < 4; ++i) glds16(ab + k * 16384 + i * 4096, offL, d + 16384 + i * 4096);
      }
    };
#pragma unroll
    for (int s = 0; s < K2_NST - 1; ++s) stage(s, s * K2_SLOT);
    unsigned nxt = (K2_NST - 1) * K2_SLOT;
    for (int t = 0; t < K2_NT; ++t) {
      const int rem = K2_NT - 1 - t;
      if (rem >= 2) WAIT_V(16); else if (rem == 1) WAIT_V(8); else WAIT_V(0);
      BAR();
      if (t + K2_NST - 1 < K2_NT) stage(t + K2_NST - 1, nxt);
      nxt = nxt + K2_SLOT == K2_LDS ? 0 : nxt + K2_SLOT;
    }
    BAR();
    if (w == 0) {
      unsigned spins = 0;
      while (__builtin_amdgcn_readfirstlane(__hip_atomic_load(flags + (tile * 4 + ks) * 64, __ATOMIC_RELAXED, __HIP_MEMORY_SCOPE_AGENT)) < 3u) {
        if (++spins > 2000000u) break;
        __builtin_amdgcn_s_sleep(1);
      }
      TS(11);
    }
  } else {
    const int wm = wid >> 1, wn = wid & 1, i16 = lane & 15, q = lane >> 4, qq = i16 >> 2, p = lane & 3;
    const unsigned bc = (64 * wm + 8 * qq + p) * 128 + ((q ^ (i16 >> 1)) << 4);
    const unsigned bh = wm * 8192 + lane * 16;
    const unsigned bab = 16384 + wn * 8192 + lane * 16;
    const int sw = (2 * q + (qq >> 1)) & 3;
    unsigned bw[4];
#pragma unroll
    for (int c = 0; c < 4; ++c) bw[c] = 16384 + wn * 8192 + (4 * q + qq) * 128 + ((c ^ sw) << 5) + 8 * p;
    f32x4 acc[4][4] = {};
    bf16x8 sA[4], eA[4], sB[4], eB[4];
    auto rd = [&](bool ffn, unsigned slot, int kk, bf16x8 (&sf)[4], bf16x8 (&ef)[4]) {
      if (ffn) {
#pragma unroll
        for (int m = 0; m < 4; ++m) sf[m] = lds128(slot + bh + m * 2048 + kk * 1024);
#pragma unroll
        for (int c = 0; c < 4; ++c) ef[c] = ldstr(slot + bw[c] + kk * 4096, 2048);
      } else {
#pragma unroll
        for (int m = 0; m < 4; ++m) sf[m] = lds128(slot + (bc ^ (kk << 6)) + (m >> 1) * 4096 + (m & 1) * 512);
#pragma unroll
        for (int c = 0; c < 4; ++c) ef[c] = lds128(slot + bab + c * 2048 + kk * 1024);
      }
    };
    auto mm = [&](bf16x8 (&sf)[4], bf16x8 (&ef)[4]) {
#pragma unroll
      for (int c = 0; c < 4; ++c)
#pragma unroll
        for (int m = 0; m < 4; ++m) acc[c][m] = MFMA(ef[c], sf[m], acc[c][m]);
    };
    unsigned cur = 0;
    BAR();
    if (wid == 0) TS(7);
    rd(true, cur, 0, sA, eA);
    auto step = [&](bool fc, bool fn, bool last) {
      rd(fc, cur, 1, sB, eB);
      SCHED();
      mm(sA, eA);
      SCHED();
      BAR_L();
      cur = cur + K2_SLOT == K2_LDS ? 0 : cur + K2_SLOT;
      if (!last) rd(fn, cur, 0, sA, eA);
      SCHED();
      mm(sB, eB);
      SCHED();
    };
    for (int t = 0; t < K2_NF - 1; ++t) step(true, true, false);
    step(true, false, false);
    if (wid == 0) TS(8);
    for (int t = 0; t < K2_NS - 1; ++t) step(false, false, false);
    step(false, false, true);
    if (wid == 0) TS(9);
    if (wid != ks) {
      const __amdgpu_buffer_rsrc_t rs = __builtin_amdgcn_make_buffer_rsrc(ws + WS_P + (size_t)((tile * 4 + wid) * 4 + ks) * 16384, 0, 16384, 0x00020000);
#pragma unroll
      for (int c = 0; c < 4; ++c)
#pragma unroll
        for (int m = 0; m < 4; ++m)
          __builtin_amdgcn_raw_buffer_store_b128(__builtin_bit_cast(u32x4, acc[c][m]), rs, (c * 4 + m) * 1024 + lane * 16, 0, 16);
      WAIT_V(0);
      if (wid == ((ks + 1) & 3)) TS(10);
      if (lane == 0) __hip_atomic_fetch_add(flags + (tile * 4 + wid) * 64, 1u, __ATOMIC_RELAXED, __HIP_MEMORY_SCOPE_AGENT);
    } else {
#pragma unroll
      for (int c = 0; c < 4; ++c)
#pragma unroll
        for (int m = 0; m < 4; ++m) *(LAS f32x4*)((c * 4 + m) * 1024 + lane * 16) = acc[c][m];
    }
  }
  BAR_L();
  if (wid == 0) TS(12);
  {
    const int i16 = lane & 15, q = lane >> 4, m = wid & 3, c0 = 2 * (wid >> 2);
    const __amdgpu_buffer_rsrc_t rs = __builtin_amdgcn_make_buffer_rsrc(ws + WS_P + (size_t)(tile * 4 + ks) * 65536, 0, 65536, 0x00020000);
    const int row = tm * 128 + 64 * (ks >> 1) + 32 * (m >> 1) + 4 * (m & 1) + 8 * (i16 >> 2) + (i16 & 3);
    const int col = tn * 128 + 64 * (ks & 1) + 16 * c0 + 4 * q;
    f32x4 v[2][3]; u32x2 xr[2];
#pragma unroll
    for (int c = 0; c < 2; ++c) {
#pragma unroll
      for (int s = 0; s < 3; ++s) {
        const int src = s + (s >= ks);
        v[c][s] = __builtin_bit_cast(f32x4, __builtin_amdgcn_raw_buffer_load_b128(rs, src * 16384 + ((c0 + c) * 4 + m) * 1024 + lane * 16, 0, 16));
      }
      xr[c] = *(const u32x2*)(x + (size_t)row * 1024 + (col + 16 * c) * 2);
    }
#pragma unroll
    for (int c = 0; c < 2; ++c) {
      f32x4 o = *(LAS f32x4*)(((c0 + c) * 4 + m) * 1024 + lane * 16);
      o = (o + v[c][0]) + (v[c][1] + v[c][2]);
      o[0] += __uint_as_float(xr[c][0] << 16); o[1] += __uint_as_float(xr[c][0] & 0xffff0000u);
      o[2] += __uint_as_float(xr[c][1] << 16); o[3] += __uint_as_float(xr[c][1] & 0xffff0000u);
      u32x2 w2 = {pkbf(o[0], o[1]), pkbf(o[2], o[3])};
      *(u32x2*)(out + (size_t)row * 1024 + (col + 16 * c) * 2) = w2;
    }
  }
  if (wid == 0) { WAIT_V(0); TS(13); }
}

extern "C" void kernel_launch(void* const* d_in, const int* in_sizes, int n_in, void* d_out, int out_size, void* d_ws, size_t ws_size, hipStream_t stream) {
  (void)hipFuncSetAttribute((const void*)k1, hipFuncAttributeMaxDynamicSharedMemorySize, K1_LDS);
  (void)hipFuncSetAttribute((const void*)k2, hipFuncAttributeMaxDynamicSharedMemorySize, K2_LDS);
  k1<<<256, 512, K1_LDS, stream>>>((const char*)d_in[0], (const char*)d_in[3], (const char*)d_in[4], (const char*)d_in[6], (const char*)d_in[7], (char*)d_ws);
  k2<<<256, 512, K2_LDS, stream>>>((const char*)d_in[0], (const char*)d_in[1], (const char*)d_in[2], (const char*)d_in[8], (char*)d_ws, (char*)d_out);
}
```

```cpp
#define SPINX 0
#define SA 0
#define SB 13
#define STAMP 15
#include <hip/hip_runtime.h>
#include <hip/hip_bf16.h>

typedef unsigned short u16;
typedef short bf16x8 __attribute__((ext_vector_type(8)));
typedef short s16x4 __attribute__((ext_vector_type(4)));
typedef float f32x4 __attribute__((ext_vector_type(4)));
typedef float f32x2 __attribute__((ext_vector_type(2)));
typedef unsigned u32x4 __attribute__((ext_vector_type(4)));
typedef unsigned u32x2 __attribute__((ext_vector_type(2)));
typedef __bf16 bfx2 __attribute__((ext_vector_type(2)));
#define LAS __attribute__((address_space(3)))
#define GAS __attribute__((address_space(1)))
#define DI __device__ __forceinline__

constexpr int S = 2048, D = 512, F = 1792;
constexpr size_t WS_HF = 0;
constexpr size_t WS_AB = WS_HF + (size_t)S * F * 2;
constexpr size_t WS_P = WS_AB + (size_t)2 * D * S * 2;
constexpr size_t WS_FLAG = WS_P + (size_t)64 * 16 * 16384;
constexpr size_t WS_TS = WS_FLAG + 65536;

#define TS(k) do { if (lane == 0) ((unsigned long long*)(ws + WS_TS))[blockIdx.x * STAMP + (k)] = __builtin_amdgcn_s_memrealtime(); } while (0)
#define WAIT_V(n) asm volatile("s_waitcnt vmcnt(%0)" ::"n"(n) : "memory")
#define BAR_L() asm volatile("s_waitcnt lgkmcnt(0)\n\ts_barrier" ::: "memory")
#define BAR() asm volatile("s_barrier" ::: "memory")
#define SCHED() __builtin_amdgcn_sched_barrier(0)
#define MFMA(a, b, c) __builtin_amdgcn_mfma_f32_16x16x32_bf16((a), (b), (c), 0, 0, 0)

DI void glds16(const char* sbase, unsigned voff, unsigned lds) {
  unsigned keep;
  asm volatile("s_mov_b32 %0, m0\n\ts_mov_b32 m0, %3\n\ts_nop 0\n\tglobal_load_lds_dwordx4 %1, %2\n\ts_mov_b32 m0, %0"
               : "=&s"(keep) : "v"(voff), "s"(sbase), "s"(lds) : "memory");
}
DI void glds4(const char* sbase, unsigned voff, unsigned lds) {
  unsigned keep;
  asm volatile("s_mov_b32 %0, m0\n\ts_mov_b32 m0, %3\n\ts_nop 0\n\tglobal_load_lds_dword %1, %2\n\ts_mov_b32 m0, %0"
               : "=&s"(keep) : "v"(voff), "s"(sbase), "s"(lds) : "memory");
}
DI bf16x8 lds128(unsigned a) { return *(const LAS bf16x8*)a; }
DI bf16x8 ldstr(unsigned a, int off2) {
  s16x4 lo = __builtin_amdgcn_ds_read_tr16_b64_v4i16((LAS s16x4*)a);
  s16x4 hi = __builtin_amdgcn_ds_read_tr16_b64_v4i16((LAS s16x4*)(a + off2));
  return __builtin_shufflevector(lo, hi, 0, 1, 2, 3, 4, 5, 6, 7);
}
DI float dot2(unsigned a, unsigned b, float c) { return __builtin_amdgcn_fdot2_f32_bf16(__builtin_bit_cast(bfx2, a), __builtin_bit_cast(bfx2, b), c, false); }
DI unsigned pkbf(float lo, float hi) { f32x2 f = {lo, hi}; return __builtin_bit_cast(unsigned, __builtin_convertvector(f, bfx2)); }
DI float rbf(float f) { return __uint_as_float(pkbf(f, 0.f) << 16); }
DI float gelu_mul(float g, float u) {
  float e = __builtin_amdgcn_exp2f(g * (-2.3022082f + -0.10294324f * g * g));
  return g * __builtin_amdgcn_rcpf(1.0f + e) * u;
}

constexpr int K1_SLOT = 53248, K1_NST = 3, K1_TAB = K1_SLOT * K1_NST, K1_LDS = 163840, K1_NT = 8;

template <int NG>
DI void k1_body(const char* x, const char* wr, const char* wi, const char* wg, const char* wu, char* ws, int rb, int g, int j, int wid, int lane) {
  constexpr int NA = 9 - 2 * NG;
  constexpr int GUB = NG * 8192;
  constexpr int ABO = 16384 + GUB;
  const int f0 = 448 * g + (NG == 4 ? 128 * j : 384);
  const int e0 = 256 * (g & 1) + (NG == 4 ? 160 + 32 * j : 0);
  LAS float* tab = (LAS float*)K1_TAB;
  if (wid >= 4) {
    const int w = wid - 4, lr = lane >> 3, lc = lane & 7;
    const unsigned offA = lr * 1024 + ((lc ^ ((w << 1) | ((lr >> 1) & 1))) << 4);
    const unsigned offG = lr * (F * 2) + ((lc ^ ((((lr >> 1) & 1) | ((w & 1) << 1)) << 1)) << 4);
    const unsigned offE = lr * 1024 + ((lc ^ (((w & 1) << 2) | (lr >> 1))) << 4);
    const char* xa = x + (size_t)(rb * 128 + 8 * w) * 1024;
    const char* ga = wg + (size_t)(8 * w) * (F * 2) + f0 * 2;
    const char* ua = wu + (size_t)(8 * w) * (F * 2) + f0 * 2;
    const char* ea = (g < 2 ? wr : wi) + (size_t)(e0 + 8 * w) * 1024;
    auto stage = [&](int kt, unsigned slot) {
      const unsigned d = slot + w * 1024;
#pragma unroll
      for (int i = 0; i < 4; ++i) glds16(xa + i * 32768 + kt * 128, offA, d + i * 4096);
#pragma unroll
      for (int i = 0; i < 2 * NG; ++i) {
        const int sub = i >> 1;
        const char* b = (NG == 4 ? (sub < 2 ? ga : ua) + (sub & 1) * 128 : (sub ? ua : ga));
        glds16(b + (size_t)(kt * 64 + (i & 1) * 32) * (F * 2), offG, d + 16384 + i * 4096);
      }
#pragma unroll
      for (int i = 0; i < NA; ++i) glds16(ea + i * 32768 + kt * 128, offE, d + ABO + i * 4096);
    };
    float sm[4] = {}, sq[4] = {}, cs[NG] = {}, ca[NA] = {};
    const int gq = lane >> 4, qq = (lane & 15) >> 2, p = lane & 3, sw2 = ((qq >> 1) & 1) | ((gq & 1) << 1);
    stage(0, 0); stage(1, K1_SLOT);
    unsigned cur = 0, nxt = 2 * K1_SLOT;
    for (int t = 0; t < K1_NT; ++t) {
      if (t + 1 < K1_NT) WAIT_V(13); else WAIT_V(0);
      BAR_L();
      if (t + 2 < K1_NT) stage(t + 2, nxt);
#pragma unroll
      for (int c = 0; c < 4; ++c) {
        u32x4 v = __builtin_bit_cast(u32x4, lds128(cur + w * 4096 + c * 1024 + lane * 16));
#pragma unroll
        for (int e = 0; e < 4; ++e) { sm[c] = dot2(v[e], 0x3f803f80u, sm[c]); sq[c] = dot2(v[e], v[e], sq[c]); }
      }
#pragma unroll
      for (int c = 0; c < NG; ++c) {
        const int ct = NG == 4 ? c : 2 * (w & 1) + c, sub = NG == 4 ? w : w >> 1;
        const unsigned a = cur + 16384 + sub * 8192 + (8 * gq + qq) * 128 + ((ct ^ sw2) << 5) + 8 * p;
#pragma unroll
        for (int kk = 0; kk < 2; ++kk) {
          u32x4 v = __builtin_bit_cast(u32x4, ldstr(a + kk * 4096, 512));
#pragma unroll
          for (int e = 0; e < 4; ++e) cs[c] = dot2(v[e], 0x3f803f80u, cs[c]);
        }
      }
#pragma unroll
      for (int i = 0; i < NA; ++i) {
        u32x4 v = __builtin_bit_cast(u32x4, lds128(cur + ABO + w * 1024 + i * 4096 + lane * 16));
#pragma unroll
        for (int e = 0; e < 4; ++e) ca[i] = dot2(v[e], 0x3f803f80u, ca[i]);
      }
      nxt = cur; cur = cur + K1_SLOT == K1_TAB ? 0 : cur + K1_SLOT;
    }
#pragma unroll
    for (int c = 0; c < 4; ++c) {
      float s = sm[c], q = sq[c];
      s += __shfl_xor(s, 1); q += __shfl_xor(q, 1); s += __shfl_xor(s, 2); q += __shfl_xor(q, 2); s += __shfl_xor(s, 4); q += __shfl_xor(q, 4);
      const float mean = s * (1.0f / D), var = fmaxf(q * (1.0f / D) - mean * mean, 0.f);
      const float sd = rbf(sqrtf(rbf(rbf(var) + 1e-5f)));
      if (lc == 0) { tab[32 * w + 8 * c + lr] = rbf(mean); tab[128 + 32 * w + 8 * c + lr] = 1.0f / sd; }
    }
#pragma unroll
    for (int c = 0; c < NG; ++c) {
      const int ct = NG == 4 ? c : 2 * (w & 1) + c, sub = NG == 4 ? w : w >> 1;
      float s = cs[c]; s += __shfl_xor(s, 16); s += __shfl_xor(s, 32);
      if (lane < 16) tab[256 + sub * 64 + ct * 16 + lane] = s;
    }
#pragma unroll
    for (int i = 0; i < NA; ++i) {
      float s = ca[i]; s += __shfl_xor(s, 1); s += __shfl_xor(s, 2); s += __shfl_xor(s, 4);
      if (lc == 0) tab[256 + NG * 64 + 8 * (w + 4 * i) + lr] = s;
    }
    BAR_L();
  } else {
    const int wm = wid >> 1, wn = wid & 1, i16 = lane & 15, q = lane >> 4, qq = i16 >> 2, p = lane & 3;
    const int sw2 = ((qq >> 1) & 1) | ((q & 1) << 1);
    const unsigned bx = (64 * wm + 8 * qq + p) * 128 + ((q ^ (i16 >> 1)) << 4);
    const unsigned be = ABO + (16 * NA * wn + i16) * 128 + ((q ^ (i16 >> 1)) << 4);
    unsigned bg[NG];
#pragma unroll
    for (int c = 0; c < NG; ++c) bg[c] = 16384 + (NG == 4 ? wn * 8192 : 0) + (8 * q + qq) * 128 + (((NG == 4 ? c : 2 * wn + c) ^ sw2) << 5) + 8 * p;
    constexpr int UPO = GUB / 2;
    f32x4 ag[4][NG] = {}, au[4][NG] = {}, aa[4][NA] = {};
    unsigned cur = 0;
    for (int t = 0; t < K1_NT; ++t) {
      BAR_L();
      if (wid == 0 && t == 0) TS(1);
#pragma unroll
      for (int kk = 0; kk < 2; ++kk) {
        bf16x8 xf[4];
#pragma unroll
        for (int m = 0; m < 4; ++m) xf[m] = lds128(cur + (bx ^ (kk << 6)) + (m >> 1) * 4096 + (m & 1) * 512);
#pragma unroll
        for (int c = 0; c < NG; ++c) {
          bf16x8 wf = ldstr(cur + bg[c] + kk * 4096, 512);
#pragma unroll
          for (int m = 0; m < 4; ++m) ag[m][c] = MFMA(wf, xf[m], ag[m][c]);
          bf16x8 uf = ldstr(cur + bg[c] + UPO + kk * 4096, 512);
#pragma unroll
          for (int m = 0; m < 4; ++m) au[m][c] = MFMA(uf, xf[m], au[m][c]);
        }
#pragma unroll
        for (int a = 0; a < NA; ++a) {
          bf16x8 ef = lds128(cur + (be ^ (kk << 6)) + a * 2048);
#pragma unroll
          for (int m = 0; m < 4; ++m) aa[m][a] = MFMA(xf[m], ef, aa[m][a]);
        }
      }
      cur = cur + K1_SLOT == K1_TAB ? 0 : cur + K1_SLOT;
    }
    if (wid == 0) TS(2);
    BAR_L();
    if (wid == 0) TS(3);
    const float sgn = g < 2 ? 1.0f : -1.0f;
#pragma unroll
    for (int sb = 0; sb < 2; ++sb) {
      const int s0 = 64 * wm + 32 * sb + 8 * q;
      const f32x4 mb0 = *(LAS f32x4*)(tab + s0), mb1 = *(LAS f32x4*)(tab + s0 + 4);
      const f32x4 r0 = *(LAS f32x4*)(tab + 128 + s0) * sgn, r1 = *(LAS f32x4*)(tab + 128 + s0 + 4) * sgn;
#pragma unroll
      for (int a = 0; a < NA; ++a) {
        const int tile = NA * wn + a;
        const float c = tab[256 + NG * 64 + 16 * tile + i16];
        const f32x4 v0 = (aa[2 * sb][a] - mb0 * c) * r0, v1 = (aa[2 * sb + 1][a] - mb1 * c) * r1;
        u32x4 o = {pkbf(v0[0], v0[1]), pkbf(v0[2], v0[3]), pkbf(v1[0], v1[1]), pkbf(v1[2], v1[3])};
        const int eb = (e0 >> 4) + tile;
        char* dst = ws + WS_AB + (size_t)(((((g >> 1) * 4 + (eb >> 3)) * 32 + 2 * rb + wm) * 8 + (eb & 7)) * 2 + sb) * 1024 + lane * 16;
        *(u32x4*)dst = o;
      }
    }
#pragma unroll
    for (int m = 0; m < 4; ++m) {
      const int sl = 64 * wm + 32 * (m >> 1) + 4 * (m & 1) + 8 * qq + p;
      const float mb = tab[sl], r = tab[128 + sl];
#pragma unroll
      for (int fb = 0; fb < NG / 2; ++fb) {
        float hv[8];
#pragma unroll
        for (int c = 0; c < 2; ++c) {
          const int fo = 16 * NG * wn + 32 * fb + 16 * c + 4 * q;
          const f32x4 cg = *(LAS f32x4*)(tab + 256 + fo), cu = *(LAS f32x4*)(tab + 256 + NG * 32 + fo);
          const f32x4 gv = (ag[m][2 * fb + c] - cg * mb) * r, uv = (au[m][2 * fb + c] - cu * mb) * r;
#pragma unroll
          for (int e = 0; e < 4; ++e) hv[4 * c + e] = gelu_mul(gv[e], uv[e]);
        }
        u32x4 o = {pkbf(hv[0], hv[1]), pkbf(hv[2], hv[3]), pkbf(hv[4], hv[5]), pkbf(hv[6], hv[7])};
        const int kstep = 7 * g + (NG == 4 ? 2 * j + wn : 6), fblk = NG == 4 ? fb : wn;
        char* dst = ws + WS_HF + (size_t)((((rb * 28 + kstep) * 4 + 2 * wm + (m >> 1)) * 2 + (m & 1)) * 2 + fblk) * 1024 + lane * 16;
        *(u32x4*)dst = o;
      }
    }
  }
}

__global__ __launch_bounds__(512, 2) void k1(const char* x, const char* wr, const char* wi, const char* wg, const char* wu, char* ws) {
  extern __shared__ __attribute__((aligned(16))) char shm[];
  const int b = blockIdx.x, xc = b & 7, r = b >> 3, g = xc >> 1, rb = (xc & 1) * 8 + (r >> 2), j = r & 3;
  const int wid = __builtin_amdgcn_readfirstlane(threadIdx.x >> 6), lane = threadIdx.x & 63;
  if (wid == 0) TS(0);
  if (threadIdx.x == 0) *(unsigned*)(ws + WS_FLAG + b * 256) = 0u;
  if (j < 3) k1_body<4>(x, wr, wi, wg, wu, ws, rb, g, j, wid, lane);
  else k1_body<2>(x, wr, wi, wg, wu, ws, rb, g, j, wid, lane);
  if (wid == 0) { TS(4); WAIT_V(0); TS(5); }
}

constexpr int K2_SLOT = 32768, K2_NST = 4, K2_LDS = K2_SLOT * K2_NST, K2_NF = 7, K2_NS = 16, K2_NT = K2_NF + K2_NS;

__global__ __launch_bounds__(512, 2) void k2(const char* x, const char* cr, const char* ci, const char* wd, char* ws, char* out) {
  extern __shared__ __attribute__((aligned(16))) char shm[];
  const int b = blockIdx.x, xc = b & 7, r = b >> 3, ks = xc >> 1, tm = (xc & 1) * 8 + (r >> 2), tn = r & 3;
  const int wid = __builtin_amdgcn_readfirstlane(threadIdx.x >> 6), lane = threadIdx.x & 63;
  const int tile = tm * 4 + tn;
  if (wid == 0) TS(6);
  GAS unsigned* flags = (GAS unsigned*)(ws + WS_FLAG);
  if (wid >= 4) {
    const int w = wid - 4, lr = lane >> 3, lc = lane & 7;
    const unsigned offC = lr * 4096 + ((lc ^ ((w << 1) | ((lr >> 1) & 1))) << 4);
    const unsigned offW = lr * 1024 + ((lc ^ (((lr >> 1) & 3) << 1)) << 4);
    const unsigned offL = lane * 16;
    const char* ca = (ks < 2 ? cr : ci) + (size_t)(tm * 128 + 8 * w) * 4096 + (ks & 1) * 2048;
    const char* ab = ws + WS_AB + (size_t)(((ks >> 1) * 4 + tn) * 32 + (ks & 1) * 16) * 16384 + w * 1024;
    const char* ha = ws + WS_HF + (size_t)(tm * 28 + ks * 7) * 16384 + w * 1024;
    const char* wa = wd + (size_t)(ks * 448 + 8 * w) * 1024 + tn * 256;
    auto stage = [&](int kt, unsigned slot) {
      const unsigned d = slot + w * 1024;
      if (kt < K2_NF) {
#pragma unroll
        for (int i = 0; i < 4; ++i) glds16(ha + kt * 16384 + i * 4096, offL, d + i * 4096);
#pragma unroll
        for (int i = 0; i < 4; ++i) glds16(wa + (kt * 64 + (i & 1) * 32) * 1024 + (i >> 1) * 128, offW, d + 16384 + i * 4096);
      } else {
        const int k = kt - K2_NF;
#pragma unroll
        for (int i = 0; i < 4; ++i) glds16(ca + i * 131072 + k * 128, offC, d + i * 4096);
#pragma unroll
        for (int i = 0; i < 4; ++i) glds16(ab + k * 16384 + i * 4096, offL, d + 16384 + i * 4096);
      }
    };
#pragma unroll
    for (int s = 0; s < K2_NST - 1; ++s) stage(s, s * K2_SLOT);
    unsigned nxt = (K2_NST - 1) * K2_SLOT;
    for (int t = 0; t < K2_NT; ++t) {
      const int rem = K2_NT - 1 - t;
      if (rem >= 2) WAIT_V(16); else if (rem == 1) WAIT_V(8); else WAIT_V(0);
      BAR();
      if (t + K2_NST - 1 < K2_NT) stage(t + K2_NST - 1, nxt);
      nxt = nxt + K2_SLOT == K2_LDS ? 0 : nxt + K2_SLOT;
    }
    BAR();
    if (w == 0) {
      unsigned spins = 0;
      while (__builtin_amdgcn_readfirstlane(__hip_atomic_load(flags + (tile * 4 + ks) * 64, __ATOMIC_RELAXED, __HIP_MEMORY_SCOPE_AGENT)) < 3u) {
        if (++spins > 2000000u) break;
        __builtin_amdgcn_s_sleep(1);
      }
      TS(11);
    }
  } else {
    const int wm = wid >> 1, wn = wid & 1, i16 = lane & 15, q = lane >> 4, qq = i16 >> 2, p = lane & 3;
    const unsigned bc = (64 * wm + 8 * qq + p) * 128 + ((q ^ (i16 >> 1)) << 4);
    const unsigned bh = wm * 8192 + lane * 16;
    const unsigned bab = 16384 + wn * 8192 + lane * 16;
    const int sw = (2 * q + (qq >> 1)) & 3;
    unsigned bw[4];
#pragma unroll
    for (int c = 0; c < 4; ++c) bw[c] = 16384 + wn * 8192 + (4 * q + qq) * 128 + ((c ^ sw) << 5) + 8 * p;
    f32x4 acc[4][4] = {};
    bf16x8 sA[4], eA[4], sB[4], eB[4];
    auto rd = [&](bool ffn, unsigned slot, int kk, bf16x8 (&sf)[4], bf16x8 (&ef)[4]) {
      if (ffn) {
#pragma unroll
        for (int m = 0; m < 4; ++m) sf[m] = lds128(slot + bh + m * 2048 + kk * 1024);
#pragma unroll
        for (int c = 0; c < 4; ++c) ef[c] = ldstr(slot + bw[c] + kk * 4096, 2048);
      } else {
#pragma unroll
        for (int m = 0; m < 4; ++m) sf[m] = lds128(slot + (bc ^ (kk << 6)) + (m >> 1) * 4096 + (m & 1) * 512);
#pragma unroll
        for (int c = 0; c < 4; ++c) ef[c] = lds128(slot + bab + c * 2048 + kk * 1024);
      }
    };
    auto mm = [&](bf16x8 (&sf)[4], bf16x8 (&ef)[4]) {
#pragma unroll
      for (int c = 0; c < 4; ++c)
#pragma unroll
        for (int m = 0; m < 4; ++m) acc[c][m] = MFMA(ef[c], sf[m], acc[c][m]);
    };
    unsigned cur = 0;
    BAR();
    if (wid == 0) TS(7);
    const unsigned long long cy0 = __builtin_amdgcn_s_memtime();
    rd(true, cur, 0, sA, eA);
    auto step = [&](bool fc, bool fn, bool last) {
      rd(fc, cur, 1, sB, eB);
      SCHED();
      mm(sA, eA);
      SCHED();
      BAR_L();
      cur = cur + K2_SLOT == K2_LDS ? 0 : cur + K2_SLOT;
      if (!last) rd(fn, cur, 0, sA, eA);
      SCHED();
      mm(sB, eB);
      SCHED();
    };
    for (int t = 0; t < K2_NF - 1; ++t) step(true, true, false);
    step(true, false, false);
    if (wid == 0) TS(8);
    for (int t = 0; t < K2_NS - 1; ++t) step(false, false, false);
    step(false, false, true);
    if (wid == 0) TS(9);
    if (wid == 0 && lane == 0) ((unsigned long long*)(ws + WS_TS))[blockIdx.x * STAMP + 14] = ((unsigned long long*)(ws + WS_TS))[0] + (__builtin_amdgcn_s_memtime() - cy0) / 10;
    if (wid != ks) {
      const __amdgpu_buffer_rsrc_t rs = __builtin_amdgcn_make_buffer_rsrc(ws + WS_P + (size_t)((tile * 4 + wid) * 4 + ks) * 16384, 0, 16384, 0x00020000);
#pragma unroll
      for (int c = 0; c < 4; ++c)
#pragma unroll
        for (int m = 0; m < 4; ++m)
          __builtin_amdgcn_raw_buffer_store_b128(__builtin_bit_cast(u32x4, acc[c][m]), rs, (c * 4 + m) * 1024 + lane * 16, 0, 16);
      WAIT_V(0);
      if (wid == ((ks + 1) & 3)) TS(10);
      if (lane == 0) __hip_atomic_fetch_add(flags + (tile * 4 + wid) * 64, 1u, __ATOMIC_RELAXED, __HIP_MEMORY_SCOPE_AGENT);
    } else {
#pragma unroll
      for (int c = 0; c < 4; ++c)
#pragma unroll
        for (int m = 0; m < 4; ++m) *(LAS f32x4*)((c * 4 + m) * 1024 + lane * 16) = acc[c][m];
    }
  }
  BAR_L();
  if (wid == 0) TS(12);
  {
    const int i16 = lane & 15, q = lane >> 4, m = wid & 3, c0 = 2 * (wid >> 2);
    const __amdgpu_buffer_rsrc_t rs = __builtin_amdgcn_make_buffer_rsrc(ws + WS_P + (size_t)(tile * 4 + ks) * 65536, 0, 65536, 0x00020000);
    const int row = tm * 128 + 64 * (ks >> 1) + 32 * (m >> 1) + 4 * (m & 1) + 8 * (i16 >> 2) + (i16 & 3);
    const int col = tn * 128 + 64 * (ks & 1) + 16 * c0 + 4 * q;
    f32x4 v[2][3]; u32x2 xr[2];
#pragma unroll
    for (int c = 0; c < 2; ++c) {
#pragma unroll
      for (int s = 0; s < 3; ++s) {
        const int src = s + (s >= ks);
        v[c][s] = __builtin_bit_cast(f32x4, __builtin_amdgcn_raw_buffer_load_b128(rs, src * 16384 + ((c0 + c) * 4 + m) * 1024 + lane * 16, 0, 16));
      }
      xr[c] = *(const u32x2*)(x + (size_t)row * 1024 + (col + 16 * c) * 2);
    }
#pragma unroll
    for (int c = 0; c < 2; ++c) {
      f32x4 o = *(LAS f32x4*)(((c0 + c) * 4 + m) * 1024 + lane * 16);
      o = (o + v[c][0]) + (v[c][1] + v[c][2]);
      o[0] += __uint_as_float(xr[c][0] << 16); o[1] += __uint_as_float(xr[c][0] & 0xffff0000u);
      o[2] += __uint_as_float(xr[c][1] << 16); o[3] += __uint_as_float(xr[c][1] & 0xffff0000u);
      u32x2 w2 = {pkbf(o[0], o[1]), pkbf(o[2], o[3])};
      *(u32x2*)(out + (size_t)row * 1024 + (col + 16 * c) * 2) = w2;
    }
  }
  if (wid == 0) { WAIT_V(0); TS(13); }
}

__global__ void spin_k(const char* ws,int a,int b){
  const unsigned long long* ts=(const unsigned long long*)(ws+WS_TS);
  unsigned long long mn=ts[threadIdx.x*STAMP+a], mx=ts[threadIdx.x*STAMP+b];
  __shared__ unsigned long long smn[256], smx[256];
  smn[threadIdx.x]=mn; smx[threadIdx.x]=mx; __syncthreads();
  if(threadIdx.x==0){ for(int i=0;i<256;++i){ mn=min(mn,smn[i]); mx=max(mx,smx[i]); }
  unsigned long long span=mx-mn; if(span>10000) span=10000;
  unsigned long long t0=__builtin_amdgcn_s_memrealtime();
  while(__builtin_amdgcn_s_memrealtime()-t0 < span*SPINX) __builtin_amdgcn_s_sleep(8); }
}
extern "C" void kernel_launch(void* const* d_in, const int* in_sizes, int n_in, void* d_out, int out_size, void* d_ws, size_t ws_size, hipStream_t stream) {
  (void)hipFuncSetAttribute((const void*)k1, hipFuncAttributeMaxDynamicSharedMemorySize, K1_LDS);
  (void)hipFuncSetAttribute((const void*)k2, hipFuncAttributeMaxDynamicSharedMemorySize, K2_LDS);
  k1<<<256, 512, K1_LDS, stream>>>((const char*)d_in[0], (const char*)d_in[3], (const char*)d_in[4], (const char*)d_in[6], (const char*)d_in[7], (char*)d_ws);
  k2<<<256, 512, K2_LDS, stream>>>((const char*)d_in[0], (const char*)d_in[1], (const char*)d_in[2], (const char*)d_in[8], (char*)d_ws, (char*)d_out);
  spin_k<<<1,256,0,stream>>>((const char*)d_ws,SA,SB);
}
```

```cpp
#include <hip/hip_runtime.h>
#include <hip/hip_bf16.h>
#include <type_traits>

typedef short bf16x8 __attribute__((ext_vector_type(8)));
typedef short s16x4 __attribute__((ext_vector_type(4)));
typedef float f32x4 __attribute__((ext_vector_type(4)));
typedef float f32x2 __attribute__((ext_vector_type(2)));
typedef unsigned u32x4 __attribute__((ext_vector_type(4)));
typedef unsigned u32x2 __attribute__((ext_vector_type(2)));
typedef __bf16 bfx2 __attribute__((ext_vector_type(2)));
#define LAS __attribute__((address_space(3)))
#define DI __device__ __forceinline__
template <int N> using IC = std::integral_constant<int, N>;

constexpr int S = 2048, D = 512, F = 1792;
constexpr unsigned WS_HF = 0;
constexpr unsigned WS_AB = WS_HF + S * F * 2;
constexpr unsigned WS_P = WS_AB + 2 * D * S * 2;
constexpr unsigned WS_EP = WS_P + 64 * 16 * 8192;
constexpr unsigned WS_ST = WS_EP + 256;
constexpr unsigned WS_CA = WS_ST + 2048 * 8;
constexpr unsigned WS_CG = WS_CA + 1024 * 8;
constexpr unsigned WS_FL = WS_CG + 2 * F * 32;
constexpr unsigned WS_FP = WS_FL + 256 * 128;
constexpr unsigned WS_END = WS_FP + 256 * 128;
constexpr unsigned WS_TS = WS_END;
constexpr unsigned SLOT = 36864, RING = 4 * SLOT, TAB = RING, SCR = TAB + 4736, DUMP = SCR + 8192, LDS_BYTES = DUMP + 1024;
constexpr int T_R = 512, T_MR = 640, T_CG = 768, T_CA = 1024;

#define TS(k)
#define WAIT_V(n) asm volatile("s_waitcnt vmcnt(%0)" ::"i"(n) : "memory")
#define WAIT_Q(mark) WAIT_V(qn - (mark))
#define BAR_L() asm volatile("s_waitcnt lgkmcnt(0)\n\ts_barrier" ::: "memory")
#define BARS() BAR_L()
#define TSFLUSH(set)
#define XS(k)
#define SCHED() __builtin_amdgcn_sched_barrier(0)
#define SGB(mask, n) __builtin_amdgcn_sched_group_barrier((mask), (n), 0)
#define MFMA(a, b, c) __builtin_amdgcn_mfma_f32_16x16x32_bf16((a), (b), (c), 0, 0, 0)

#define GLDS4X(SC) "s_mov_b32 m0, %5\n\ts_nop 0\n\tglobal_load_lds_dwordx4 %0, %4" SC "\n\tglobal_load_lds_dwordx4 %1, %4 offset:1024" SC \
                   "\n\tglobal_load_lds_dwordx4 %2, %4 offset:2048" SC "\n\tglobal_load_lds_dwordx4 %3, %4 offset:3072" SC
DI void glds4x(const char* sbase, const unsigned (&v)[4], unsigned lds) {
  asm volatile(GLDS4X("") :: "v"(v[0]), "v"(v[1]), "v"(v[2]), "v"(v[3]), "s"(sbase), "s"(lds) : "memory");
}
DI void glds4x_dev(const char* sbase, unsigned v, unsigned lds) {
  asm volatile(GLDS4X(" sc1") :: "v"(v), "v"(v), "v"(v), "v"(v), "s"(sbase), "s"(lds) : "memory");
}
DI void glds1x(const char* sbase, unsigned voff, unsigned lds) {
  asm volatile("s_mov_b32 m0, %2\n\ts_nop 0\n\tglobal_load_lds_dwordx4 %0, %1" :: "v"(voff), "s"(sbase), "s"(lds) : "memory");
}
DI void glds4(const char* sbase, unsigned voff, unsigned lds) {
  asm volatile("s_mov_b32 m0, %2\n\ts_nop 0\n\tglobal_load_lds_dword %0, %1" :: "v"(voff), "s"(sbase), "s"(lds) : "memory");
}
DI u32x4 gld16(const char* sbase, unsigned voff) { u32x4 r; asm volatile("global_load_dwordx4 %0, %1, %2" : "=v"(r) : "v"(voff), "s"(sbase) : "memory"); return r; }
DI u32x4 gld16d(const char* sbase, unsigned voff) { u32x4 r; asm volatile("global_load_dwordx4 %0, %1, %2 sc1" : "=v"(r) : "v"(voff), "s"(sbase) : "memory"); return r; }
DI u32x2 gld8(const char* sbase, unsigned voff) { u32x2 r; asm volatile("global_load_dwordx2 %0, %1, %2" : "=v"(r) : "v"(voff), "s"(sbase) : "memory"); return r; }
DI unsigned gld4d(const char* sbase, unsigned voff) { unsigned r; asm volatile("global_load_dword %0, %1, %2 sc1" : "=v"(r) : "v"(voff), "s"(sbase) : "memory"); return r; }
DI void gst16d(char* sbase, unsigned voff, u32x4 v) { asm volatile("global_store_dwordx4 %0, %1, %2 sc1\n\ts_nop 1" :: "v"(voff), "v"(v), "s"(sbase) : "memory"); }
DI void gst8d(char* sbase, unsigned voff, u32x2 v) { asm volatile("global_store_dwordx2 %0, %1, %2 sc1" :: "v"(voff), "v"(v), "s"(sbase) : "memory"); }
DI void gst4d(char* sbase, unsigned voff, unsigned v) { asm volatile("global_store_dword %0, %1, %2 sc1" :: "v"(voff), "v"(v), "s"(sbase) : "memory"); }
#define PIN(x) asm volatile("" : "+v"(x))
#define PINS(x) asm("" : "+v"(x))

typedef LAS char* lptr;
DI lptr LP(unsigned a) { return (lptr)(size_t)a; }
DI bf16x8 lds128(lptr a) { return *(const LAS bf16x8*)a; }
DI bf16x8 ldstr(lptr a, int off2) {
  s16x4 lo = __builtin_amdgcn_ds_read_tr16_b64_v4i16((LAS s16x4*)a);
  s16x4 hi = __builtin_amdgcn_ds_read_tr16_b64_v4i16((LAS s16x4*)(a + off2));
  return __builtin_shufflevector(lo, hi, 0, 1, 2, 3, 4, 5, 6, 7);
}
DI unsigned pkbf(float lo, float hi) { f32x2 f = {lo, hi}; return __builtin_bit_cast(unsigned, __builtin_convertvector(f, bfx2)); }
DI float rbf(float f) { return __uint_as_float(pkbf(f, 0.f) << 16); }
DI float blo(unsigned v) { return __uint_as_float(v << 16); }
DI float bhi(unsigned v) { return __uint_as_float(v & 0xffff0000u); }
template <int CTRL, int RM> DI float dppadd(float v) { return v + __builtin_bit_cast(float, __builtin_amdgcn_update_dpp(0, __builtin_bit_cast(int, v), CTRL, RM, 0xf, true)); }
DI float wsum63(float v) {
  v = dppadd<0x111, 0xf>(v); v = dppadd<0x112, 0xf>(v); v = dppadd<0x114, 0xf>(v); v = dppadd<0x118, 0xf>(v); v = dppadd<0x142, 0xa>(v); return dppadd<0x143, 0xc>(v);
}
DI bool expired(unsigned long long t0) { return __builtin_amdgcn_s_memrealtime() - t0 > 5000000ull; }

enum { P1G = 0, P1H = 1, P2 = 2, SP = 3, FF = 4, NONE = 5 };
constexpr int kNE(int k) { return k <= P1H ? 5 : 4; }
constexpr int kNR(int k) { return k == P1G ? 13 : k == P1H ? 9 : k == SP ? 8 : 12; }

#define PFX 7
#define PFY 17
#define PRIO_C 1
#define PRIO_L 0
struct Args { const char *x, *cr, *ci, *wr, *wi, *wg, *wu, *wd; char *ws, *out; };

template <bool H, bool SETB>
DI void body(const Args& A, const int wid) {
  const int b = blockIdx.x, xc = b & 7, r = b >> 3, g = xc >> 1, mh = xc & 1, rb = mh * 8 + (r >> 2), j = r & 3;
  const int lane = threadIdx.x & 63, w = wid & 3;
  char* const ws = A.ws;
  const int f2 = 448 * g + (H ? 384 : 128 * j + 64), f1 = H ? f2 : f2 - 64;
  const int e0 = 256 * (g & 1) + (H ? 0 : 160 + 32 * j);
  const int tile = rb * 4 + j;
  LAS float* const tab = (LAS float*)(size_t)TAB;
  int qn = 0;
  if (wid == 0) TS(0);

  const int lr = lane >> 3, lc = lane & 7;
  unsigned vX[4], vC[4], vG[4], vW[4], vE[4], vE1;
  const char *xa, *ga, *ea, *ca, *ab, *ha, *wa;
  auto init1 = [&]() {
#pragma unroll
    for (int i = 0; i < 4; ++i) {
      vX[i] = (8 * i + lr) * 1024 + ((lc ^ ((i << 1) | ((lr >> 1) & 1))) << 4) - i * 1024;
      vG[i] = (8 * i + lr) * (F * 2) + ((lc ^ ((((lr >> 1) & 1) | ((i & 1) << 1)) << 1)) << 4) - i * 1024;
      vE[i] = (8 * i + lr) * 1024 + ((lc ^ (((i & 1) << 2) | (lr >> 1))) << 4) - i * 1024;
    }
    vE1 = lr * 1024 + ((lc ^ (((w & 1) << 2) | (lr >> 1))) << 4);
    xa = A.x + (size_t)(rb * 128 + 32 * w) * 1024;
    ga = ((w >> 1) ? A.wu : A.wg) + (size_t)(32 * (w & 1)) * (F * 2);
    ea = (g < 2 ? A.wr : A.wi) + (size_t)e0 * 1024;
  };
  auto init2 = [&]() {
#pragma unroll
    for (int i = 0; i < 4; ++i) {
      vC[i] = (8 * i + lr) * 4096 + ((lc ^ ((i << 1) | ((lr >> 1) & 1))) << 4) - i * 1024;
      vW[i] = (8 * i + lr) * 1024 + ((lc ^ (((lr >> 1) & 3) << 1)) << 4) - i * 1024;
    }
    ca = (g < 2 ? A.cr : A.ci) + (size_t)(rb * 128 + 32 * w) * 4096 + (g & 1) * 2048;
    ab = ws + WS_AB + (size_t)((((g >> 1) * 4 + j) * 32 + (g & 1) * 16) * 16384 + w * 4096);
    ha = ws + WS_HF + (size_t)((rb * 28 + g * 7) * 16384 + w * 4096);
    wa = A.wd + (size_t)(g * 448 + 32 * (w & 1)) * 1024 + j * 256 + (w >> 1) * 128;
  };
  auto partS = [&](auto KIND, int kt, unsigned slot) {
    constexpr int K = KIND;
    const unsigned d = slot + w * 4096;
    if constexpr (K <= P2) glds4x(xa + kt * 128, vX, d);
    else if constexpr (K == SP) glds4x(ca + kt * 128, vC, d);
    else glds4x_dev(ha + kt * 16384, lane * 16, d);
    qn += 4;
  };
  auto partE = [&](auto KIND, int kt, unsigned slot) {
    constexpr int K = KIND;
    const unsigned d = slot + 16384 + w * 4096;
    if constexpr (K == P1H) glds4x(ea + w * 32768 + kt * 128, vE, d);
    else if constexpr (K <= P2) glds4x(ga + (size_t)kt * 64 * (F * 2) + (K == P2 ? f2 : f1) * 2, vG, d);
    else if constexpr (K == SP) glds4x_dev(ab + kt * 16384, lane * 16, d);
    else glds4x(wa + kt * 65536, vW, d);
    qn += 4;
    if constexpr (K <= P1H) { glds1x(ea + (K == P1H ? 131072 : 0) + w * 8192 + kt * 128, vE1, slot + 32768 + w * 1024); qn += 1; }
  };
  auto stage = [&](auto KIND, int kt, unsigned slot) { partS(KIND, kt, slot); partE(KIND, kt, slot); };
  unsigned w256 = w * 256;
  asm volatile("" : "+s"(w256));
  auto warmC = [&](int half) {
    const char* cb = (g < 2 ? A.cr : A.ci) + (size_t)(rb * 128 + 32 * j) * 4096 + (g & 1) * 2048 + half * 1024 + w256;
    glds4(cb, (lane & 31) * 4096 + (lane >> 5) * 128, DUMP + w256);
    qn += 1;
  };
  auto warmW = [&]() {
    glds4(A.wd + (size_t)g * 448 * 1024 + r * 14336 + w256 * 14, min(lane, 27) * 128, DUMP + w256);
    qn += 1;
  };

  const int wm = w >> 1, wn = w & 1, i16 = lane & 15, q = lane >> 4, qq = i16 >> 2, p4 = lane & 3;
  auto rd = [&](auto KIND, unsigned slot, int kk, bf16x8 (&sf)[4], bf16x8 (&ef)[5]) {
    constexpr int K = KIND;
    if constexpr (K == FF) {
#pragma unroll
      for (int m = 0; m < 4; ++m) sf[m] = lds128(LP(slot + wm * 8192 + lane * 16) + m * 2048 + kk * 1024);
      const int sw = (2 * q + (qq >> 1)) & 3;
#pragma unroll
      for (int c = 0; c < 4; ++c) ef[c] = ldstr(LP(slot + 16384 + wn * 8192 + (4 * q + qq) * 128 + ((c ^ sw) << 5) + 8 * p4) + kk * 4096, 2048);
    } else {
      const unsigned bx = (64 * wm + 8 * qq + p4) * 128 + ((q ^ (i16 >> 1)) << 4);
#pragma unroll
      for (int m = 0; m < 4; ++m) sf[m] = lds128(LP(slot + (bx ^ (kk << 6))) + (m >> 1) * 4096 + (m & 1) * 512);
      if constexpr (K == SP) {
#pragma unroll
        for (int c = 0; c < 4; ++c) ef[c] = lds128(LP(slot + 16384 + wn * 8192 + lane * 16) + c * 2048 + kk * 1024);
      } else {
        if constexpr (K != P1H) {
          const int sw2 = ((qq >> 1) & 1) | ((q & 1) << 1);
#pragma unroll
          for (int c = 0; c < 4; ++c)
            ef[c] = ldstr(LP(slot + 16384 + (8 * q + qq) * 128 + (((2 * wn + (c & 1)) ^ sw2) << 5) + 8 * p4) + (c >> 1) * 8192 + kk * 4096, 512);
        }
        if constexpr (K != P2) {
          const unsigned be = (K == P1H ? 16384 + 80 * wn * 128 : 32768 + 16 * wn * 128) + i16 * 128 + ((q ^ (i16 >> 1)) << 4);
#pragma unroll
          for (int c = (K == P1H ? 0 : 4); c < 5; ++c) ef[c] = lds128(LP(slot + (be ^ (kk << 6))) + (K == P1H ? c : 0) * 2048);
        }
      }
    }
  };
  f32x4 acc[5][4];
  bf16x8 sA[4], eA[5], sB[4], eB[5];
  auto mm = [&](auto KIND, bf16x8 (&sf)[4], bf16x8 (&ef)[5]) {
    constexpr int K = KIND;
#pragma unroll
    for (int c = 0; c < kNE(K); ++c)
#pragma unroll
      for (int m = 0; m < 4; ++m) {
        if (K == P1H || (K == P1G && c == 4)) acc[c][m] = MFMA(sf[m], ef[c], acc[c][m]);
        else acc[c][m] = MFMA(ef[c], sf[m], acc[c][m]);
      }
  };
  unsigned cur = 0;
  auto step = [&](auto KC, auto KN, auto DOW, int wv) {
    constexpr int kc = KC, kn = KN;
    rd(KC, cur, 1, sB, eB);
    mm(KC, sA, eA);
#pragma unroll
    for (int u = 0; u < kNR(kc); ++u) { SGB(0x008, 1); SGB(0x100, 1); }
    SGB(0x008, 4 * kNE(kc) - kNR(kc));
    SCHED();
    if constexpr ((int)DOW) WAIT_V(wv);
    BAR_L();
    cur = cur + SLOT == RING ? 0 : cur + SLOT;
    if constexpr (kn != NONE) rd(KN, cur, 0, sA, eA);
    mm(KC, sB, eB);
    if constexpr (kn != NONE) {
#pragma unroll
      for (int u = 0; u < kNR(kn); ++u) { SGB(0x008, 1); SGB(0x100, 1); }
      SGB(0x008, 4 * kNE(kc) - kNR(kn));
    }
    SCHED();
  };
  auto zero_acc = [&]() {
#pragma unroll
    for (int c = 0; c < 5; ++c)
#pragma unroll
      for (int m = 0; m < 4; ++m) acc[c][m] = (f32x4){0.f, 0.f, 0.f, 0.f};
  };
  unsigned T;
  auto epi_ab = [&](int sb, int a0, int a1) {
    const int s0 = 64 * wm + 32 * sb + 8 * q;
    const f32x4 r0 = *(LAS f32x4*)(tab + T_R + s0), r1 = *(LAS f32x4*)(tab + T_R + s0 + 4);
    const f32x4 m0 = *(LAS f32x4*)(tab + T_MR + s0), m1 = *(LAS f32x4*)(tab + T_MR + s0 + 4);
#pragma unroll
    for (int a = a0; a < a1; ++a) {
      const int tl = H ? 5 * wn + a : wn, eb = (e0 >> 4) + tl;
      const float c = -tab[T_CA + 16 * tl + i16];
      float v[8];
#pragma unroll
      for (int i = 0; i < 4; ++i) {
        v[i] = __builtin_fmaf(m0[i], c, acc[a][2 * sb][i] * r0[i]); v[4 + i] = __builtin_fmaf(m1[i], c, acc[a][2 * sb + 1][i] * r1[i]);
        PINS(v[i]); PINS(v[4 + i]);
      }
      const u32x4 o = {pkbf(v[0], v[1]), pkbf(v[2], v[3]), pkbf(v[4], v[5]), pkbf(v[6], v[7])};
      gst16d(ws + (WS_AB + (((((g >> 1) * 4 + (eb >> 3)) * 32 + 2 * rb + wm) * 8 + (eb & 7)) * 2 + sb) * 1024), lane * 16, o);
      qn += 1;
    }
  };
  f32x4 rc[4], cg[2], cu[2];
  u32x4 ho;
  auto epi_load = [&](int pass) {
#pragma unroll
    for (int m = 0; m < 4; ++m) rc[m] = *(LAS f32x4*)(tab + 4 * (64 * wm + 32 * (m >> 1) + 4 * (m & 1) + 8 * qq + p4));
#pragma unroll
    for (int c = 0; c < 2; ++c) { cg[c] = *(LAS f32x4*)(tab + T_CG + 128 * pass + 32 * wn + 16 * c + 4 * q); cu[c] = *(LAS f32x4*)(tab + T_CG + 128 * pass + 64 + 32 * wn + 16 * c + 4 * q); }
#pragma unroll
    for (int m = 0; m < 4; ++m) PIN(rc[m]);
    PIN(cg[0]); PIN(cg[1]); PIN(cu[0]); PIN(cu[1]);
  };
  auto epi_h = [&](int hf, int pass) {
    const int m = hf >> 1, c = hf & 1;
    PIN(acc[c][m]); PIN(acc[2 + c][m]);
    float hv[4];
#pragma unroll
    for (int i = 0; i < 4; ++i) {
      const float gv = __builtin_fmaf(cg[c][i], rc[m][0], acc[c][m][i]);
      float uv = __builtin_fmaf(cu[c][i], rc[m][0], acc[2 + c][m][i]);
      PINS(uv);
      const float e = __builtin_amdgcn_exp2f(gv * __builtin_fmaf(gv * gv, rc[m][2], rc[m][1]));
      hv[i] = (gv * uv) * __builtin_amdgcn_rcpf(__builtin_fmaf(e, rc[m][3], rc[m][3]));
      PINS(hv[i]);
    }
    ho[2 * c] = pkbf(hv[0], hv[1]); ho[2 * c + 1] = pkbf(hv[2], hv[3]);
    if (c) {
      const int kstep = (pass ? f2 : f1) >> 6;
      gst16d(ws + (WS_HF + ((((rb * 28 + kstep) * 4 + 2 * wm + (m >> 1)) * 2 + (m & 1)) * 2 + wn) * 1024), lane * 16, ho);
      qn += 1;
    } else PIN(ho);
  };
  auto flag = [&](unsigned off) { if (lane == 0) gst4d(ws, off, T); qn += 1; };
  auto await4 = [&](u32x4 v, unsigned voff) {
    if (__all(v[0] == T && v[1] == T && v[2] == T && v[3] == T)) return;
    const unsigned long long t0 = __builtin_amdgcn_s_memrealtime();
    for (;;) {
      v = gld16d(ws, voff); WAIT_V(0); PIN(v);
      if (__all(v[0] == T && v[1] == T && v[2] == T && v[3] == T) || expired(t0)) return;
      __builtin_amdgcn_s_sleep(1);
    }
  };
  constexpr int K1 = H ? P1H : P1G;
#define NOW IC<0>{}, 0
#define STAG(N, LOADS, WORK) do { const int q0_ = qn; if (w < 2) { LOADS; } qn = q0_; SCHED(); WORK; SCHED(); \
                                  const int q1_ = qn; if (w >= 2) { LOADS; } qn = q1_ + (N); } while (0)

  const int un = min(mh * 32 + r, 55), umat = un / 28, ucb = (un % 28) >> 2, ukq = un & 3;
  if constexpr (!SETB) {
    {
      const int row0 = rb * 128 + 8 * (4 * g + j) + 2 * w, ecol = 256 * g + 4 * (mh * 32 + r) + w;
      u32x4 px0 = gld16(A.x + (size_t)row0 * 1024, lane * 16), px1 = gld16(A.x + (size_t)row0 * 1024 + 1024, lane * 16);
      u32x4 pw = gld16((g < 2 ? A.wr : A.wi) + (size_t)(ecol & 511) * 1024, lane * 16);
      unsigned ep = gld4d(ws, WS_EP);
      zero_acc();
      WAIT_V(0); PIN(px0); PIN(px1); PIN(pw); PIN(ep);
      T = __builtin_amdgcn_readfirstlane(ep) * 2654435761u + 12345u;
      float sx0 = 0.f, qx0 = 0.f, sx1 = 0.f, qx1 = 0.f, sw = 0.f;
#pragma unroll
      for (int e = 0; e < 4; ++e) {
        const float a = blo(px0[e]), a2 = bhi(px0[e]), c = blo(px1[e]), c2 = bhi(px1[e]);
        sx0 += a + a2; qx0 += a * a + a2 * a2; sx1 += c + c2; qx1 += c * c + c2 * c2; sw += blo(pw[e]) + bhi(pw[e]);
      }
      sx0 = wsum63(sx0); qx0 = wsum63(qx0); sx1 = wsum63(sx1); qx1 = wsum63(qx1); sw = wsum63(sw);
      const float mean0 = sx0 * (1.0f / D), var0 = fmaxf(qx0 * (1.0f / D) - mean0 * mean0, 0.f);
      const float mean1 = sx1 * (1.0f / D), var1 = fmaxf(qx1 * (1.0f / D) - mean1 * mean1, 0.f);
      const unsigned vb = pkbf(var0, var1), ve = pkbf(blo(vb) + 1e-5f, bhi(vb) + 1e-5f);
      u32x4 gs = {pkbf(mean0, __builtin_amdgcn_sqrtf(blo(ve))), T, pkbf(mean1, __builtin_amdgcn_sqrtf(bhi(ve))), T};
      u32x2 gw = {__float_as_uint(sw), T};
      if (lane == 63) { gst16d(ws, WS_ST + row0 * 8, gs); gst8d(ws, WS_CA + ecol * 8, gw); }
    }
    __builtin_amdgcn_s_setprio(PRIO_C);
    BAR_L();
    if (wid == 0) TS(1);
    __builtin_amdgcn_s_waitcnt(0xC07F);
    rd(IC<K1>{}, cur, 0, sA, eA);
#pragma unroll 1
    for (int t = 0; t < 7; ++t) step(IC<K1>{}, IC<K1>{}, NOW);
    step(IC<K1>{}, IC<NONE>{}, NOW);
    if (wid == 0) TS(2);
    const unsigned pollAB = WS_FL + (((((lane >> 2) & 7) * 4 + ((j & 1) ? (lane & 3) : 3)) * 8 + (2 * (g >> 1) + (j >> 1)) * 2 + (g & 1)) * 128);
    __builtin_amdgcn_s_setprio(2);
    init1();
    if constexpr (H) STAG(8, stage(IC<P2>{}, 3, 3 * SLOT), epi_ab(0, 0, 5));
    else STAG(8, stage(IC<P2>{}, 3, 3 * SLOT), { epi_ab(0, 4, 5); epi_ab(1, 4, 5); });
    const int s11 = qn;
    BARS();
    if constexpr (H) { STAG(8, stage(IC<P2>{}, 4, 0), epi_ab(1, 0, 5)); __builtin_amdgcn_s_setprio(PRIO_L); }
    else { __builtin_amdgcn_s_setprio(PRIO_L); STAG(8, stage(IC<P2>{}, 4, 0), { epi_load(0); epi_h(0, 0); }); }
    const int s12 = qn;
    BARS();
    STAG(8, stage(IC<P2>{}, 5, SLOT), if constexpr (!H) epi_h(1, 0)); const int s13 = qn;
    WAIT_Q(s11);
    if constexpr (!H) flag(WS_FL + b * 128 + w * 4);
    BARS();
    STAG(8, stage(IC<P2>{}, 6, 2 * SLOT), if constexpr (!H) epi_h(2, 0)); const int s14 = qn;
    WAIT_Q(s12);
    if constexpr (H) flag(WS_FL + b * 128 + w * 4);
    if (wid == 0) TS(3);
    BARS();
    STAG(8, stage(IC<P2>{}, 7, 3 * SLOT), if constexpr (!H) epi_h(3, 0));
    warmC(1); warmW();
    WAIT_Q(s13); BARS();
    init2();
    if constexpr (!H) { epi_h(4, 0); epi_h(5, 0); }
    WAIT_Q(s14); BARS();
    u32x4 pv = gld16d(ws, pollAB); qn += 1; const int mpoll = qn;
    partS(IC<SP>{}, 0, 0);
    partS(IC<SP>{}, 1, SLOT);
    if constexpr (!H) { epi_h(6, 0); epi_h(7, 0); }
    WAIT_Q(mpoll); PIN(pv);
    await4(pv, pollAB);
    if (wid == 0) TS(4);
    partE(IC<SP>{}, 0, 0); const int s16 = qn;
    BARS();
    partE(IC<SP>{}, 1, SLOT); const int s17 = qn;
    stage(IC<SP>{}, 2, 2 * SLOT); const int s18 = qn;
    zero_acc();
    WAIT_Q(s16); flag(WS_FL + b * 128 + 16 + w * 4);
    BARS();
    if (wid == 0) TS(5);
    TSFLUSH(0);
    __builtin_amdgcn_s_setprio(PRIO_C);
    cur = 0;
    __builtin_amdgcn_s_waitcnt(0xC07F);
    rd(IC<SP>{}, cur, 0, sA, eA);
    step(IC<SP>{}, IC<SP>{}, IC<1>{}, qn - s17);
    step(IC<SP>{}, IC<SP>{}, IC<1>{}, qn - s18);
#pragma unroll 1
    for (int t = 2; t < 15; ++t) step(IC<SP>{}, IC<SP>{}, NOW);
    step(IC<SP>{}, IC<FF>{}, NOW);
    if (wid == 0) TS(6);
#pragma unroll 1
    for (int t = 0; t < 6; ++t) step(IC<FF>{}, IC<FF>{}, NOW);
    step(IC<FF>{}, IC<NONE>{}, NOW);
    if (wid == 0) TS(7);
    if (w != g) {
#pragma unroll
      for (int c = 0; c < 2; ++c)
#pragma unroll
        for (int m = 0; m < 4; ++m) {
          const f32x4 a = acc[2 * c][m], a2 = acc[2 * c + 1][m];
          const u32x4 o = {pkbf(a[0], a[1]), pkbf(a[2], a[3]), pkbf(a2[0], a2[1]), pkbf(a2[2], a2[3])};
          gst16d(ws + (WS_P + ((tile * 4 + w) * 4 + g) * 8192 + (c * 4 + m) * 1024), lane * 16, o);
        }
      BAR_L();
      WAIT_V(0);
      flag(WS_FP + (tile * 4 + w) * 128 + g * 4);
      if (w == ((g + 1) & 3)) TS(8);
    } else {
#pragma unroll
      for (int c = 0; c < 4; ++c)
#pragma unroll
        for (int m = 0; m < 4; ++m) *(LAS f32x4*)(LP(lane * 16) + (c * 4 + m) * 1024) = acc[c][m];
      BAR_L();
    }
  } else {
    init1();
    stage(IC<K1>{}, 0, 0); const int s0_ = qn;
    u32x4 pg[4];
    {
      const char* ub = (umat ? A.wu : A.wg) + (size_t)(ukq * 128 + 32 * w) * (F * 2) + (448 * g + 64 * ucb) * 2;
#pragma unroll
      for (int i = 0; i < 4; ++i) pg[i] = gld16(ub + i * 8 * (F * 2), lr * (F * 2) + lc * 16);
    }
    unsigned ep = gld4d(ws, WS_EP); qn += 5; const int mpg = qn;
    partS(IC<K1>{}, 1, SLOT);
    WAIT_Q(s0_);
    BARS();
    partE(IC<K1>{}, 1, SLOT); const int s1_ = qn;
    partS(IC<K1>{}, 2, 2 * SLOT);
    WAIT_Q(mpg); PIN(ep); PIN(pg[0]); PIN(pg[1]); PIN(pg[2]); PIN(pg[3]);
    T = __builtin_amdgcn_readfirstlane(ep) * 2654435761u + 12345u;
    {
      float c8[8] = {};
#pragma unroll
      for (int e = 0; e < 4; ++e)
#pragma unroll
        for (int i = 0; i < 4; ++i) { c8[2 * e] += blo(pg[i][e]); c8[2 * e + 1] += bhi(pg[i][e]); }
#pragma unroll
      for (int e = 0; e < 8; ++e) PINS(c8[e]);
      *(LAS f32x4*)(LP(SCR + (w * 8 + lr) * 256 + lc * 32)) = (f32x4){c8[0], c8[1], c8[2], c8[3]};
      *(LAS f32x4*)(LP(SCR + (w * 8 + lr) * 256 + lc * 32) + 16) = (f32x4){c8[4], c8[5], c8[6], c8[7]};
    }
    WAIT_Q(s1_); BARS();
    partE(IC<K1>{}, 2, 2 * SLOT); const int s2_ = qn;
    stage(IC<K1>{}, 3, 3 * SLOT); const int s3_ = qn;
    WAIT_Q(s2_); BARS();
    {
      float s = 0.f;
#pragma unroll
      for (int i = 0; i < 8; ++i) s += *(LAS float*)(LP(SCR + q * 2048 + (16 * w + i16) * 4) + i * 256);
      stage(IC<K1>{}, 4, 0);
      s += __shfl_xor(s, 16); s += __shfl_xor(s, 32);
      const u32x2 gr = {__float_as_uint(s), T};
      if (lane < 16) gst8d(ws, WS_CG + ((umat * F + 448 * g + 64 * ucb + 16 * w + lane) * 4 + ukq) * 8, gr);
    }
    const int s4_ = qn; qn += 1;
    partS(IC<K1>{}, 5, SLOT);
    WAIT_Q(s3_); BARS();
    partE(IC<K1>{}, 5, SLOT); const int s5_ = qn;
    stage(IC<K1>{}, 6, 2 * SLOT); const int s6_ = qn;
    WAIT_Q(s4_); BARS();
    stage(IC<K1>{}, 7, 3 * SLOT);
    warmC(0);
    WAIT_Q(s5_); BARS();
    constexpr int NAL = H ? 20 : 4;
    const unsigned ga0 = lane < 16 ? WS_ST + (rb * 128 + 32 * w + 2 * lane) * 8 : WS_CA + (512 * (g >> 1) + e0 + 2 * NAL * w + 2 * min(lane - 16, NAL - 1)) * 8;
    const unsigned ga1 = WS_CG + ((w & 1) * F + ((w >> 1) ? f2 : f1) + lane) * 32;
    zero_acc();
    u32x4 t0 = gld16d(ws, ga0), t1 = gld16d(ws, ga1), t2 = gld16d(ws, ga1 + 16); qn += 3; const int mga = qn;
    WAIT_Q(s6_); BARS();
    stage(IC<P2>{}, 0, 0); const int s8_ = qn;
    partS(IC<P2>{}, 1, SLOT);
    WAIT_Q(mga); PIN(t0); PIN(t1); PIN(t2);
    if (!__all(t0[1] == T && t0[3] == T && t1[1] == T && t1[3] == T && t2[1] == T && t2[3] == T)) {
      const unsigned long long c0 = __builtin_amdgcn_s_memrealtime();
      do {
        t0 = gld16d(ws, ga0); t1 = gld16d(ws, ga1); t2 = gld16d(ws, ga1 + 16);
        WAIT_V(0); PIN(t0); PIN(t1); PIN(t2);
      } while (!__all(t0[1] == T && t0[3] == T && t1[1] == T && t1[3] == T && t2[1] == T && t2[3] == T) && !expired(c0));
    }
    tab[T_CG + 64 * w + lane] = (__uint_as_float(t1[0]) + __uint_as_float(t1[2])) + (__uint_as_float(t2[0]) + __uint_as_float(t2[2]));
    if (lane >= 16 && lane < 16 + NAL) *(LAS f32x2*)(tab + T_CA + 2 * NAL * w + 2 * (lane - 16)) = (f32x2){__uint_as_float(t0[0]), __uint_as_float(t0[2])};
    BARS();
    partE(IC<P2>{}, 1, SLOT); const int s9_ = qn;
    stage(IC<P2>{}, 2, 2 * SLOT); const int s10_ = qn;
    PIN(t0);
    {
      const unsigned st = (unsigned)__builtin_amdgcn_ds_bpermute((lane >> 1) * 4, (int)t0[0]);
      const unsigned st2 = (unsigned)__builtin_amdgcn_ds_bpermute((lane >> 1) * 4, (int)t0[2]);
      const unsigned sv = (lane & 1) ? st2 : st;
      const float sgn = g < 2 ? 1.0f : -1.0f, m0 = blo(sv), d0 = bhi(sv), r0 = __builtin_amdgcn_rcpf(d0);
      if (lane < 32) {
        const int rw = 32 * w + lane;
        *(LAS f32x4*)(tab + 4 * rw) = (f32x4){-m0, -2.3022082f * r0, -0.10294324f * r0 * r0 * r0, d0 * d0};
        tab[T_R + rw] = r0 * sgn;
        tab[T_MR + rw] = m0 * r0 * sgn;
      }
    }
    WAIT_Q(s8_); BARS();
    __builtin_amdgcn_s_setprio(PRIO_C);
    __builtin_amdgcn_s_waitcnt(0xC07F);
    rd(IC<P2>{}, cur, 0, sA, eA);
    step(IC<P2>{}, IC<P2>{}, IC<1>{}, qn - s9_);
    step(IC<P2>{}, IC<P2>{}, IC<1>{}, qn - s10_);
#pragma unroll 1
    for (int t = 2; t < 7; ++t) step(IC<P2>{}, IC<P2>{}, NOW);
    step(IC<P2>{}, IC<NONE>{}, NOW);
    __builtin_amdgcn_s_setprio(PRIO_L);
    int sp[13];
    init2();
    stage(IC<SP>{}, 3, 3 * SLOT); sp[0] = qn;
    epi_load(1);
    BARS();
#pragma unroll
    for (int k = 1; k <= 8; ++k) {
      STAG(8, stage(IC<SP>{}, 3 + k, ((3 + k) & 3) * SLOT), epi_h(k - 1, 1)); sp[k] = qn;
      if (k >= 2) WAIT_Q(sp[k - 2]);
      BARS();
    }
    const int mh2 = qn;
    const int row = rb * 128 + 64 * (g >> 1) + 32 * (w >> 1) + 4 * (w & 1) + 8 * qq + p4, col = j * 128 + 64 * (g & 1) + 4 * q;
    stage(IC<SP>{}, 12, 0); sp[9] = qn;
    WAIT_Q(sp[7]); BARS();
    stage(IC<SP>{}, 13, SLOT); sp[10] = qn;
    WAIT_Q(mh2); flag(WS_FL + b * 128 + 32 + w * 4);
    BARS();
    const unsigned pollH = WS_FL + (((r & ~3) + (lane & 3)) * 8 + xc) * 128 + 16;
    stage(IC<SP>{}, 14, 2 * SLOT); sp[11] = qn;
    u32x4 pv = gld16d(ws, pollH); qn += 1; const int mp1 = qn;
    WAIT_Q(sp[9]); BARS();
    stage(IC<SP>{}, 15, 3 * SLOT); sp[12] = qn;
    WAIT_Q(sp[10]); BARS();
    WAIT_Q(mp1); PIN(pv); await4(pv, pollH);
    stage(IC<FF>{}, 0, 0); const int f0_ = qn;
    BARS();
    stage(IC<FF>{}, 2, SLOT); const int f1_ = qn;
    pv = gld16d(ws, pollH + 16); qn += 1; const int mp2 = qn;
    WAIT_Q(sp[12]); BARS();
    stage(IC<FF>{}, 4, 2 * SLOT); const int f2_ = qn;
    WAIT_Q(f0_); BARS();
    WAIT_Q(mp2); PIN(pv); await4(pv, pollH + 16);
    stage(IC<FF>{}, 1, 3 * SLOT);
    BARS();
    stage(IC<FF>{}, 3, 0);
    WAIT_Q(f2_); BARS();
    stage(IC<FF>{}, 5, SLOT); WAIT_V(16); BARS();
    stage(IC<FF>{}, 6, 2 * SLOT); WAIT_V(16); BARS();
    (void)f1_;
    u32x2 xr[4];
#pragma unroll
    for (int c = 0; c < 4; ++c) xr[c] = gld8(A.x, row * 1024 + (col + 16 * c) * 2);
    WAIT_V(12); BARS();
    WAIT_V(4); BARS();
    if (b == 0 && w == 0) {
      const unsigned long long c0 = __builtin_amdgcn_s_memrealtime();
      for (;;) {
        unsigned v[4];
#pragma unroll
        for (int i = 0; i < 4; ++i) v[i] = gld4d(ws, WS_FL + (i * 64 + lane) * 128);
        WAIT_V(0); PIN(v[0]); PIN(v[1]); PIN(v[2]); PIN(v[3]);
        if (__all(v[0] == T && v[1] == T && v[2] == T && v[3] == T) || expired(c0)) break;
      }
      flag(WS_EP);
    }
    BARS();
    BARS();
    if (w == 0) TS(9);
    {
      const unsigned pf = WS_FP + (tile * 4 + g) * 128;
      const unsigned long long c0 = __builtin_amdgcn_s_memrealtime();
      for (;;) {
        u32x4 v = gld16d(ws, pf); WAIT_V(0); PIN(v);
        if (__all((v[0] == T || g == 0) && (v[1] == T || g == 1) && (v[2] == T || g == 2) && (v[3] == T || g == 3)) || expired(c0)) break;
      }
      PIN(xr[0]); PIN(xr[1]); PIN(xr[2]); PIN(xr[3]);
      if (w == 0) TS(10);
      u32x4 v[2][3];
#pragma unroll
      for (int cp = 0; cp < 2; ++cp)
#pragma unroll
        for (int s = 0; s < 3; ++s) v[cp][s] = gld16d(ws, WS_P + ((tile * 4 + g) * 4 + s + (s >= g)) * 8192 + (cp * 4 + w) * 1024 + lane * 16);
      f32x4 o[4];
#pragma unroll
      for (int c = 0; c < 4; ++c) {
        o[c] = *(LAS f32x4*)(LP(w * 1024 + lane * 16) + c * 4096);
        o[c] += (f32x4){blo(xr[c][0]), bhi(xr[c][0]), blo(xr[c][1]), bhi(xr[c][1])};
      }
      WAIT_V(0);
#pragma unroll
      for (int cp = 0; cp < 2; ++cp)
#pragma unroll
        for (int s = 0; s < 3; ++s) PIN(v[cp][s]);
#pragma unroll
      for (int c = 0; c < 4; ++c) {
#pragma unroll
        for (int s = 0; s < 3; ++s) {
          const unsigned lo = v[c >> 1][s][2 * (c & 1)], hi = v[c >> 1][s][2 * (c & 1) + 1];
          o[c] += (f32x4){blo(lo), bhi(lo), blo(hi), bhi(hi)};
        }
        const u32x2 w2 = {pkbf(o[c][0], o[c][1]), pkbf(o[c][2], o[c][3])};
        *(u32x2*)(A.out + (size_t)row * 1024 + (col + 16 * c) * 2) = w2;
      }
      if (w == 0) { TS(11); WAIT_V(0); TS(12); }
      TSFLUSH(1);
    }
  }
}

__global__ __launch_bounds__(512, 2) void fused(Args A) {
  const int wid = __builtin_amdgcn_readfirstlane(threadIdx.x >> 6);
  if (((blockIdx.x >> 3) & 3) == 3) { if (wid >= 4) body<true, true>(A, wid); else body<true, false>(A, wid); }
  else { if (wid >= 4) body<false, true>(A, wid); else body<false, false>(A, wid); }
}

extern "C" void kernel_launch(void* const* d_in, const int* in_sizes, int n_in, void* d_out, int out_size, void* d_ws, size_t ws_size, hipStream_t stream) {
  (void)hipFuncSetAttribute((const void*)fused, hipFuncAttributeMaxDynamicSharedMemorySize, LDS_BYTES);
  Args a = {(const char*)d_in[0], (const char*)d_in[1], (const char*)d_in[2], (const char*)d_in[3], (const char*)d_in[4],
            (const char*)d_in[6], (const char*)d_in[7], (const char*)d_in[8], (char*)d_ws, (char*)d_out};
  fused<<<256, 512, LDS_BYTES, stream>>>(a);
}
```
